```python
import torch

def _softmax(x, axis=-1):
    return torch.softmax(x, dim=axis)
def _gelu(x):
    return torch.nn.functional.gelu(x, approximate="tanh")
def _silu(x):
    return torch.nn.functional.silu(x)
def _sigmoid(x):
    return torch.sigmoid(x)
def _layernorm(x, axis=-1, eps=1e-5):
    m = x.mean(dim=axis, keepdim=True)
    v = x.var(dim=axis, keepdim=True, unbiased=False)
    return (x - m) / torch.sqrt(v + eps)
def _scatter_add(tgt, idx, src, axis=0):
    return tgt.clone().index_add_(axis, idx, src)
def _segment_sum(src, seg, n):
    out = torch.zeros((n,) + src.shape[1:], dtype=src.dtype)
    return out.index_add_(0, seg, src)
def _pad(x, pw):
    flat = tuple(v for pair in reversed(pw) for v in pair)
    return torch.nn.functional.pad(x, flat)

kernel_name = 'standalone__mha_softmax__near_ridge'

def setup_inputs():

    _w = (lambda a: (lambda t: t.to(torch.bfloat16) if t.is_floating_point() else t)(torch.as_tensor(a)))
    B, H, Sq, Sk, D = 8, 16, 496, 1024, 256
    return {
        "q": _w((lambda *s: torch.randn(*s, dtype=torch.bfloat16) * 0.8)(B, H, Sq, D)),
        "k": _w((lambda *s: torch.randn(*s, dtype=torch.bfloat16) * 0.8)(B, H, Sk, D)),
        "v": _w((lambda *s: torch.randn(*s, dtype=torch.bfloat16) * 0.05)(B, H, Sk, D)),
    }

def reference(q, k, v):
    scale = q.shape[-1] ** -0.5
    scores = torch.einsum("bhqd,bhkd->bhqk", q, k) * scale
    p = _softmax(scores, axis=-1)
    return torch.einsum("bhqk,bhkd->bhqd", p, v)
```

```cpp
#include <hip/hip_runtime.h>
#include <hip/hip_bf16.h>
constexpr int B=8,H=16,SQ=496,SK=1024,D=256;
__device__ __forceinline__ float b2f(unsigned short x){ return __uint_as_float(((unsigned)x)<<16); }
__global__ __launch_bounds__(256) void naive_k(const unsigned short* q,const unsigned short* k,const unsigned short* v,__hip_bfloat16* o){
  __shared__ float s[SK]; __shared__ float qs[D]; __shared__ float red[256];
  int row=blockIdx.x; int bh=row/SQ; int t=threadIdx.x;
  qs[t]=b2f(q[(size_t)row*D+t]); __syncthreads();
  float mx=-1e30f;
  for(int j=t;j<SK;j+=256){ const unsigned short* kr=k+((size_t)bh*SK+j)*D; float a=0; for(int d=0;d<D;d++) a+=qs[d]*b2f(kr[d]); a*=0.0625f; s[j]=a; mx=fmaxf(mx,a);}
  red[t]=mx; __syncthreads(); for(int o2=128;o2>0;o2>>=1){ if(t<o2) red[t]=fmaxf(red[t],red[t+o2]); __syncthreads(); }
  mx=red[0]; __syncthreads(); float sum=0;
  for(int j=t;j<SK;j+=256){ float e=__expf(s[j]-mx); s[j]=e; sum+=e; }
  red[t]=sum; __syncthreads(); for(int o2=128;o2>0;o2>>=1){ if(t<o2) red[t]+=red[t+o2]; __syncthreads(); }
  sum=red[0]; float a=0; for(int j=0;j<SK;j++) a+=s[j]*b2f(v[((size_t)bh*SK+j)*D+t]);
  o[(size_t)row*D+t]=__float2bfloat16(a/sum);
}
extern "C" void kernel_launch(void* const* d_in, const int* in_sizes, int n_in, void* d_out, int out_size, void* d_ws, size_t ws_size, hipStream_t stream) {
  naive_k<<<B*H*SQ,256,0,stream>>>((const unsigned short*)d_in[0],(const unsigned short*)d_in[1],(const unsigned short*)d_in[2],(__hip_bfloat16*)d_out);
}
```

```cpp
#include <hip/hip_runtime.h>
#include <hip/hip_bf16.h>

typedef unsigned u32x4 __attribute__((ext_vector_type(4)));
typedef unsigned u32x2 __attribute__((ext_vector_type(2)));
constexpr int SQ = 496, SK = 1024, HD = 256;

__device__ __forceinline__ float bf_lo(unsigned x) { return __uint_as_float(x << 16); }
__device__ __forceinline__ float bf_hi(unsigned x) { return __uint_as_float(x & 0xffff0000u); }
__device__ __forceinline__ unsigned lane_id() { unsigned l; asm volatile("v_mbcnt_lo_u32_b32 %0, -1, 0\n\tv_mbcnt_hi_u32_b32 %0, -1, %0" : "=v"(l)); return l; }
__device__ __forceinline__ float bperm_f(unsigned src_lane, float x) { return __int_as_float(__builtin_amdgcn_ds_bpermute((int)(src_lane << 2), __float_as_int(x))); }

__global__ __launch_bounds__(256, 1) void mha_fwd_kernel(const unsigned short* __restrict__ Q, const unsigned short* __restrict__ K,
                                                         const unsigned short* __restrict__ V, unsigned short* __restrict__ O) {
  __shared__ __attribute__((aligned(1024))) char lds[163840];
  const unsigned blk = blockIdx.x;
  const unsigned wv = __builtin_amdgcn_readfirstlane(threadIdx.x >> 6);
  const unsigned long long kargs = (unsigned long long)__builtin_amdgcn_kernarg_segment_ptr();
  unsigned flag;
  asm volatile(
      "s_load_dwordx8 s[36:43], %1, 0x0\n"
      "s_and_b32 s44, %2, 7\n"
      "s_lshr_b32 s45, %2, 4\n"
      "s_lshl_b32 s47, s45, 3\n"
      "s_add_u32 s44, s44, s47\n"
      "s_lshr_b32 s46, %2, 3\n"
      "s_and_b32 s46, s46, 1\n"
      "s_lshl_b32 s47, s44, 19\n"
      "s_mul_i32 s55, s44, 253952\n"
      "s_mov_b32 s22, 0x80000\n"
      "s_mov_b32 s23, 0x20000\n"
      "s_lshl_b32 s92, %3, 12\n"
      "v_mbcnt_lo_u32_b32 v121, -1, 0\n"
      "v_mbcnt_hi_u32_b32 v121, -1, v121\n"
      "v_bfe_u32 v122, v121, 2, 3\n"
      "v_lshlrev_b32 v122, 9, v122\n"
      "v_lshrrev_b32 v123, 5, v121\n"
      "v_lshl_add_u32 v122, v123, 6, v122\n"
      "v_and_b32 v123, 3, v121\n"
      "s_and_b32 s48, %3, 1\n"
      "s_lshl_b32 s48, s48, 1\n"
      "v_xor_b32 v124, s48, v123\n"
      "v_lshl_add_u32 v119, v124, 4, v122\n"
      "v_add_u32 v119, s92, v119\n"
      "s_mov_b32 s30, 253952\n"
      "s_mov_b32 s31, 0x20000\n"
      "s_lshl_b32 s49, s46, 2\n"
      "s_add_u32 s49, s49, %3\n"
      "s_lshl_b32 s49, s49, 15\n"
      "s_lshl_b32 s93, %3, 13\n"
      "v_lshl_add_u32 v108, v123, 4, v122\n"
      "v_add_u32 v108, s49, v108\n"
      "v_xor_b32 v109, 32, v108\n"
      "v_add_u32 v109, 4096, v109\n"
      "v_lshlrev_b32 v36, 4, v121\n"
      "s_getpc_b64 s[50:51]\n"
      "PCT%=:\n"
      "s_mul_i32 s54, %3, 2048\n"
      "s_add_u32 s50, s50, s54\n"
      "s_addc_u32 s51, s51, 0\n"
      "global_load_dwordx4 a[252:255], v36, s[50:51] offset:0\n"
      "global_load_dwordx4 a[252:255], v36, s[50:51] offset:1024\n"
      "s_waitcnt lgkmcnt(0)\n"
      "s_add_u32 s20, s38, s47\n"
      "s_addc_u32 s21, s39, 0\n"
      "s_and_b32 s21, s21, 0xffff\n"
      "s_add_u32 s28, s36, s55\n"
      "s_addc_u32 s29, s37, 0\n"
      "s_and_b32 s29, s29, 0xffff\n"
      "s_add_u32 m0, s92, 0\n"
      "s_nop 0\n"
      "buffer_load_dwordx4 v119, s[20:23], 0 offen offset:0 nt lds\n"
      "s_add_u32 m0, s92, 896\n"
      "s_nop 0\n"
      "buffer_load_dwordx4 v119, s[20:23], 0 offen offset:128 nt lds\n"
      "s_add_u32 m0, s92, 1792\n"
      "s_nop 0\n"
      "buffer_load_dwordx4 v119, s[20:23], 0 offen offset:256 nt lds\n"
      "s_add_u32 m0, s92, 2688\n"
      "s_nop 0\n"
      "buffer_load_dwordx4 v119, s[20:23], 0 offen offset:384 nt lds\n"
      "s_add_u32 m0, s93, 65536\n"
      "s_nop 0\n"
      "buffer_load_dwordx4 v108, s[28:31], 0 offen offset:0 nt lds\n"
      "s_add_u32 m0, s93, 69632\n"
      "s_nop 0\n"
      "buffer_load_dwordx4 v109, s[28:31], 0 offen offset:0 nt lds\n"
      "s_add_u32 m0, s93, 66432\n"
      "s_nop 0\n"
      "buffer_load_dwordx4 v108, s[28:31], 0 offen offset:128 nt lds\n"
      "s_add_u32 m0, s93, 70528\n"
      "s_nop 0\n"
      "buffer_load_dwordx4 v109, s[28:31], 0 offen offset:128 nt lds\n"
      "s_add_u32 m0, s93, 67328\n"
      "s_nop 0\n"
      "buffer_load_dwordx4 v108, s[28:31], 0 offen offset:256 nt lds\n"
      "s_add_u32 m0, s93, 71424\n"
      "s_nop 0\n"
      "buffer_load_dwordx4 v109, s[28:31], 0 offen offset:256 nt lds\n"
      "s_add_u32 m0, s93, 68224\n"
      "s_nop 0\n"
      "buffer_load_dwordx4 v108, s[28:31], 0 offen offset:384 nt lds\n"
      "v_add_u32 v108, 8192, v108\n"
      "s_add_u32 m0, s93, 72320\n"
      "s_nop 0\n"
      "buffer_load_dwordx4 v109, s[28:31], 0 offen offset:384 nt lds\n"
      "v_add_u32 v109, 8192, v109\n"
      "s_add_u32 m0, s93, 32768\n"
      "s_nop 0\n"
      "buffer_load_dwordx4 v108, s[28:31], 0 offen offset:0 nt lds\n"
      "s_add_u32 m0, s93, 36864\n"
      "s_nop 0\n"
      "buffer_load_dwordx4 v109, s[28:31], 0 offen offset:0 nt lds\n"
      "s_add_u32 s24, s40, s47\n"
      "s_addc_u32 s25, s41, 0\n"
      "s_and_b32 s25, s25, 0xffff\n"
      "s_mov_b32 s26, 0x80000\n"
      "s_mov_b32 s27, 0x20000\n"
      "s_add_u32 s56, s42, s55\n"
      "s_addc_u32 s57, s43, 0\n"
      "s_and_b32 s57, s57, 0xffff\n"
      "s_mov_b32 s58, 253952\n"
      "s_mov_b32 s59, 0x20000\n"
      "s_mov_b32 s40, 0x3db8aa3b\n"
      "s_cmp_ge_u32 s49, 229376\n"
      "s_cselect_b32 s41, 0x7149f2ca, 0\n"
      "s_mov_b32 s53, 0xffff0000\n"
      "s_mov_b32 s43, 0x3f803f80\n"
      "v_bfe_u32 v124, v121, 4, 1\n"
      "v_lshlrev_b32 v124, 1, v124\n"
      "v_xor_b32 v124, v124, v123\n"
      "v_lshl_add_u32 v120, v124, 4, v122\n"
      "v_add_u32 v120, s92, v120\n"
      "v_bfe_u32 v122, v121, 3, 1\n"
      "v_lshrrev_b32 v124, 4, v121\n"
      "v_lshlrev_b32 v116, 1, v122\n"
      "v_xor_b32 v116, v116, v124\n"
      "v_lshlrev_b32 v116, 4, v116\n"
      "v_lshl_add_u32 v116, v122, 12, v116\n"
      "v_and_b32 v122, 7, v121\n"
      "v_lshl_add_u32 v116, v122, 6, v116\n"
      "v_lshrrev_b32 v122, 5, v121\n"
      "v_lshlrev_b32 v117, 12, v122\n"
      "v_bfe_u32 v122, v121, 2, 3\n"
      "v_lshl_add_u32 v117, v122, 6, v117\n"
      "v_bfe_u32 v122, v121, 4, 1\n"
      "v_lshl_add_u32 v117, v122, 5, v117\n"
      "v_lshl_add_u32 v117, v123, 3, v117\n"
      "v_add_u32 v117, 0x10000, v117\n"
      "v_xor_b32 v118, 32, v117\n"
      "s_add_u32 s52, s93, 65536\n"
      "v_add_u32 v32, s52, v116\n"
      "s_add_u32 s52, s93, 32768\n"
      "v_add_u32 v33, s52, v116\n"
      "s_add_u32 s52, s93, 98304\n"
      "v_add_u32 v34, s52, v116\n"
      "s_add_u32 s52, s93, 131072\n"
      "v_add_u32 v35, s52, v116\n"
      "v_lshlrev_b32 v36, 4, v121\n"
      "s_getpc_b64 s[50:51]\n"
      "PC0%=:\n"
      "s_add_u32 s62, s50, TAIL%=-PC0%=\n"
      "s_addc_u32 s63, s51, 0\n"
      "s_add_u32 s50, s50, MAIN%=-PC0%=\n"
      "s_addc_u32 s51, s51, 0\n"
      "s_mul_i32 s55, %3, 4096\n"
      "s_add_u32 s50, s50, s55\n"
      "s_addc_u32 s51, s51, 0\n"
      "s_mul_i32 s55, %3, 4352\n"
      "s_add_u32 s62, s62, s55\n"
      "s_addc_u32 s63, s63, 0\n"
      "s_waitcnt vmcnt(10)\n"
      "s_barrier\n"
      "ds_read_b128 a[128:131], v116 offset:0\n"
      "ds_read_b128 a[132:135], v116 offset:512\n"
      "ds_read_b128 a[136:139], v116 offset:1024\n"
      "ds_read_b128 a[140:143], v116 offset:1536\n"
      "ds_read_b128 a[144:147], v116 offset:2048\n"
      "ds_read_b128 a[148:151], v116 offset:2560\n"
      "ds_read_b128 a[152:155], v116 offset:3072\n"
      "ds_read_b128 a[156:159], v116 offset:3584\n"
      "ds_read_b128 a[160:163], v116 offset:8192\n"
      "ds_read_b128 a[164:167], v116 offset:8704\n"
      "ds_read_b128 a[168:171], v116 offset:9216\n"
      "ds_read_b128 a[172:175], v116 offset:9728\n"
      "ds_read_b128 a[176:179], v116 offset:10240\n"
      "ds_read_b128 a[180:183], v116 offset:10752\n"
      "ds_read_b128 a[184:187], v116 offset:11264\n"
      "ds_read_b128 a[188:191], v116 offset:11776\n"
      "s_waitcnt vmcnt(8)\n"
      "ds_read_b128 v[128:131], v32 offset:0\n"
      "ds_read_b128 v[132:135], v32 offset:512\n"
      "s_add_u32 m0, s93, 33664\n"
      "s_nop 0\n"
      "buffer_load_dwordx4 v108, s[28:31], 0 offen offset:128 nt lds\n"
      "s_add_u32 m0, s93, 37760\n"
      "s_nop 0\n"
      "buffer_load_dwordx4 v109, s[28:31], 0 offen offset:128 nt lds\n"
      "s_waitcnt vmcnt(8)\n"
      "ds_read_b128 v[136:139], v32 offset:1024\n"
      "ds_read_b128 v[140:143], v32 offset:1536\n"
      "s_waitcnt lgkmcnt(2)\n"
      "v_lshlrev_b32 v40, 16, v128\n"
      "v_and_b32 v41, s53, v128\n"
      "v_lshlrev_b32 v42, 16, v129\n"
      "v_and_b32 v43, s53, v129\n"
      "v_mul_f32 v40, s40, v40\n"
      "v_mul_f32 v41, s40, v41\n"
      "v_mul_f32 v42, s40, v42\n"
      "v_mul_f32 v43, s40, v43\n"
      "v_cvt_pk_bf16_f32 v128, v40, v41\n"
      "v_cvt_pk_bf16_f32 v129, v42, v43\n"
      "v_accvgpr_write_b32 a0, v128\n"
      "v_accvgpr_write_b32 a1, v129\n"
      "v_lshlrev_b32 v40, 16, v130\n"
      "v_and_b32 v41, s53, v130\n"
      "v_lshlrev_b32 v42, 16, v131\n"
      "v_and_b32 v43, s53, v131\n"
      "v_mul_f32 v40, s40, v40\n"
      "v_mul_f32 v41, s40, v41\n"
      "v_mul_f32 v42, s40, v42\n"
      "v_mul_f32 v43, s40, v43\n"
      "v_cvt_pk_bf16_f32 v130, v40, v41\n"
      "v_cvt_pk_bf16_f32 v131, v42, v43\n"
      "v_accvgpr_write_b32 a2, v130\n"
      "v_accvgpr_write_b32 a3, v131\n"
      "v_lshlrev_b32 v40, 16, v132\n"
      "v_and_b32 v41, s53, v132\n"
      "v_lshlrev_b32 v42, 16, v133\n"
      "v_and_b32 v43, s53, v133\n"
      "v_mul_f32 v40, s40, v40\n"
      "v_mul_f32 v41, s40, v41\n"
      "v_mul_f32 v42, s40, v42\n"
      "v_mul_f32 v43, s40, v43\n"
      "v_cvt_pk_bf16_f32 v132, v40, v41\n"
      "v_cvt_pk_bf16_f32 v133, v42, v43\n"
      "v_accvgpr_write_b32 a4, v132\n"
      "v_accvgpr_write_b32 a5, v133\n"
      "v_lshlrev_b32 v40, 16, v134\n"
      "v_and_b32 v41, s53, v134\n"
      "v_lshlrev_b32 v42, 16, v135\n"
      "v_and_b32 v43, s53, v135\n"
      "v_mul_f32 v40, s40, v40\n"
      "v_mul_f32 v41, s40, v41\n"
      "v_mul_f32 v42, s40, v42\n"
      "v_mul_f32 v43, s40, v43\n"
      "v_cvt_pk_bf16_f32 v134, v40, v41\n"
      "v_cvt_pk_bf16_f32 v135, v42, v43\n"
      "v_accvgpr_write_b32 a6, v134\n"
      "v_accvgpr_write_b32 a7, v135\n"
      "s_add_u32 m0, s93, 34560\n"
      "s_nop 0\n"
      "buffer_load_dwordx4 v108, s[28:31], 0 offen offset:256 nt lds\n"
      "s_add_u32 m0, s93, 38656\n"
      "s_nop 0\n"
      "buffer_load_dwordx4 v109, s[28:31], 0 offen offset:256 nt lds\n"
      "s_waitcnt vmcnt(8)\n"
      "ds_read_b128 v[144:147], v32 offset:2048\n"
      "ds_read_b128 v[148:151], v32 offset:2560\n"
      "s_waitcnt lgkmcnt(2)\n"
      "v_lshlrev_b32 v40, 16, v136\n"
      "v_and_b32 v41, s53, v136\n"
      "v_lshlrev_b32 v42, 16, v137\n"
      "v_and_b32 v43, s53, v137\n"
      "v_mfma_f32_16x16x32_bf16 v[0:3], a[128:131], v[128:131], 0\n"
      "v_mul_f32 v40, s40, v40\n"
      "v_mul_f32 v41, s40, v41\n"
      "v_mfma_f32_16x16x32_bf16 a[208:211], a[136:139], a[132:135], 0\n"
      "v_mul_f32 v42, s40, v42\n"
      "v_mul_f32 v43, s40, v43\n"
      "v_cvt_pk_bf16_f32 v136, v40, v41\n"
      "v_cvt_pk_bf16_f32 v137, v42, v43\n"
      "v_accvgpr_write_b32 a8, v136\n"
      "v_accvgpr_write_b32 a9, v137\n"
      "v_lshlrev_b32 v40, 16, v138\n"
      "v_and_b32 v41, s53, v138\n"
      "v_lshlrev_b32 v42, 16, v139\n"
      "v_and_b32 v43, s53, v139\n"
      "v_mfma_f32_16x16x32_bf16 v[0:3], a[132:135], v[132:135], v[0:3]\n"
      "v_mul_f32 v40, s40, v40\n"
      "v_mul_f32 v41, s40, v41\n"
      "v_mfma_f32_16x16x32_bf16 a[208:211], a[136:139], a[132:135], 0\n"
      "v_mul_f32 v42, s40, v42\n"
      "v_mul_f32 v43, s40, v43\n"
      "v_cvt_pk_bf16_f32 v138, v40, v41\n"
      "v_cvt_pk_bf16_f32 v139, v42, v43\n"
      "v_accvgpr_write_b32 a10, v138\n"
      "v_accvgpr_write_b32 a11, v139\n"
      "v_lshlrev_b32 v40, 16, v140\n"
      "v_and_b32 v41, s53, v140\n"
      "v_lshlrev_b32 v42, 16, v141\n"
      "v_and_b32 v43, s53, v141\n"
      "v_mfma_f32_16x16x32_bf16 v[4:7], a[160:163], v[128:131], 0\n"
      "v_mul_f32 v40, s40, v40\n"
      "v_mul_f32 v41, s40, v41\n"
      "v_mfma_f32_16x16x32_bf16 a[208:211], a[140:143], a[132:135], 0\n"
      "v_mul_f32 v42, s40, v42\n"
      "v_mul_f32 v43, s40, v43\n"
      "v_cvt_pk_bf16_f32 v140, v40, v41\n"
      "v_cvt_pk_bf16_f32 v141, v42, v43\n"
      "v_accvgpr_write_b32 a12, v140\n"
      "v_accvgpr_write_b32 a13, v141\n"
      "v_lshlrev_b32 v40, 16, v142\n"
      "v_and_b32 v41, s53, v142\n"
      "v_lshlrev_b32 v42, 16, v143\n"
      "v_and_b32 v43, s53, v143\n"
      "v_mfma_f32_16x16x32_bf16 v[4:7], a[164:167], v[132:135], v[4:7]\n"
      "v_mul_f32 v40, s40, v40\n"
      "v_mul_f32 v41, s40, v41\n"
      "v_mfma_f32_16x16x32_bf16 a[208:211], a[140:143], a[132:135], 0\n"
      "v_mul_f32 v42, s40, v42\n"
      "v_mul_f32 v43, s40, v43\n"
      "v_cvt_pk_bf16_f32 v142, v40, v41\n"
      "v_cvt_pk_bf16_f32 v143, v42, v43\n"
      "v_accvgpr_write_b32 a14, v142\n"
      "v_accvgpr_write_b32 a15, v143\n"
      "s_add_u32 m0, s93, 35456\n"
      "s_nop 0\n"
      "buffer_load_dwordx4 v108, s[28:31], 0 offen offset:384 nt lds\n"
      "v_add_u32 v108, 8192, v108\n"
      "s_add_u32 m0, s93, 39552\n"
      "s_nop 0\n"
      "buffer_load_dwordx4 v109, s[28:31], 0 offen offset:384 nt lds\n"
      "v_add_u32 v109, 8192, v109\n"
      "s_waitcnt vmcnt(8)\n"
      "ds_read_b128 v[152:155], v32 offset:3072\n"
      "ds_read_b128 v[156:159], v32 offset:3584\n"
      "s_waitcnt lgkmcnt(2)\n"
      "v_lshlrev_b32 v40, 16, v144\n"
      "v_and_b32 v41, s53, v144\n"
      "v_lshlrev_b32 v42, 16, v145\n"
      "v_and_b32 v43, s53, v145\n"
      "v_mfma_f32_16x16x32_bf16 v[0:3], a[136:139], v[136:139], v[0:3]\n"
      "v_mul_f32 v40, s40, v40\n"
      "v_mul_f32 v41, s40, v41\n"
      "v_mfma_f32_16x16x32_bf16 a[224:227], a[144:147], a[136:139], 0\n"
      "v_mul_f32 v42, s40, v42\n"
      "v_mul_f32 v43, s40, v43\n"
      "v_cvt_pk_bf16_f32 v144, v40, v41\n"
      "v_cvt_pk_bf16_f32 v145, v42, v43\n"
      "v_accvgpr_write_b32 a16, v144\n"
      "v_accvgpr_write_b32 a17, v145\n"
      "v_lshlrev_b32 v40, 16, v146\n"
      "v_and_b32 v41, s53, v146\n"
      "v_lshlrev_b32 v42, 16, v147\n"
      "v_and_b32 v43, s53, v147\n"
      "v_mfma_f32_16x16x32_bf16 v[0:3], a[140:143], v[140:143], v[0:3]\n"
      "v_mul_f32 v40, s40, v40\n"
      "v_mul_f32 v41, s40, v41\n"
      "v_mfma_f32_16x16x32_bf16 a[224:227], a[144:147], a[136:139], 0\n"
      "v_mul_f32 v42, s40, v42\n"
      "v_mul_f32 v43, s40, v43\n"
      "v_cvt_pk_bf16_f32 v146, v40, v41\n"
      "v_cvt_pk_bf16_f32 v147, v42, v43\n"
      "v_accvgpr_write_b32 a18, v146\n"
      "v_accvgpr_write_b32 a19, v147\n"
      "v_lshlrev_b32 v40, 16, v148\n"
      "v_and_b32 v41, s53, v148\n"
      "v_lshlrev_b32 v42, 16, v149\n"
      "v_and_b32 v43, s53, v149\n"
      "v_mfma_f32_16x16x32_bf16 v[4:7], a[168:171], v[136:139], v[4:7]\n"
      "v_mul_f32 v40, s40, v40\n"
      "v_mul_f32 v41, s40, v41\n"
      "v_mfma_f32_16x16x32_bf16 a[224:227], a[148:151], a[136:139], 0\n"
      "v_mul_f32 v42, s40, v42\n"
      "v_mul_f32 v43, s40, v43\n"
      "v_cvt_pk_bf16_f32 v148, v40, v41\n"
      "v_cvt_pk_bf16_f32 v149, v42, v43\n"
      "v_accvgpr_write_b32 a20, v148\n"
      "v_accvgpr_write_b32 a21, v149\n"
      "v_lshlrev_b32 v40, 16, v150\n"
      "v_and_b32 v41, s53, v150\n"
      "v_lshlrev_b32 v42, 16, v151\n"
      "v_and_b32 v43, s53, v151\n"
      "v_mfma_f32_16x16x32_bf16 v[4:7], a[172:175], v[140:143], v[4:7]\n"
      "v_mul_f32 v40, s40, v40\n"
      "v_mul_f32 v41, s40, v41\n"
      "v_mfma_f32_16x16x32_bf16 a[224:227], a[148:151], a[136:139], 0\n"
      "v_mul_f32 v42, s40, v42\n"
      "v_mul_f32 v43, s40, v43\n"
      "v_cvt_pk_bf16_f32 v150, v40, v41\n"
      "v_cvt_pk_bf16_f32 v151, v42, v43\n"
      "v_accvgpr_write_b32 a22, v150\n"
      "v_accvgpr_write_b32 a23, v151\n"
      "s_add_u32 m0, s93, 98304\n"
      "s_nop 0\n"
      "buffer_load_dwordx4 v108, s[28:31], 0 offen offset:0 nt lds\n"
      "s_add_u32 m0, s93, 102400\n"
      "s_nop 0\n"
      "buffer_load_dwordx4 v109, s[28:31], 0 offen offset:0 nt lds\n"
      "s_waitcnt vmcnt(8)\n"
      "ds_read_b128 v[160:163], v33 offset:0\n"
      "ds_read_b128 v[164:167], v33 offset:512\n"
      "s_waitcnt lgkmcnt(2)\n"
      "v_lshlrev_b32 v40, 16, v152\n"
      "v_and_b32 v41, s53, v152\n"
      "v_lshlrev_b32 v42, 16, v153\n"
      "v_and_b32 v43, s53, v153\n"
      "v_mfma_f32_16x16x32_bf16 v[0:3], a[144:147], v[144:147], v[0:3]\n"
      "v_mul_f32 v40, s40, v40\n"
      "v_mul_f32 v41, s40, v41\n"
      "v_mfma_f32_16x16x32_bf16 a[240:243], a[152:155], a[140:143], 0\n"
      "v_mul_f32 v42, s40, v42\n"
      "v_mul_f32 v43, s40, v43\n"
      "v_cvt_pk_bf16_f32 v152, v40, v41\n"
      "v_cvt_pk_bf16_f32 v153, v42, v43\n"
      "v_accvgpr_write_b32 a24, v152\n"
      "v_accvgpr_write_b32 a25, v153\n"
      "v_lshlrev_b32 v40, 16, v154\n"
      "v_and_b32 v41, s53, v154\n"
      "v_lshlrev_b32 v42, 16, v155\n"
      "v_and_b32 v43, s53, v155\n"
      "v_mfma_f32_16x16x32_bf16 v[0:3], a[148:151], v[148:151], v[0:3]\n"
      "v_mul_f32 v40, s40, v40\n"
      "v_mul_f32 v41, s40, v41\n"
      "v_mfma_f32_16x16x32_bf16 a[240:243], a[152:155], a[140:143], 0\n"
      "v_mul_f32 v42, s40, v42\n"
      "v_mul_f32 v43, s40, v43\n"
      "v_cvt_pk_bf16_f32 v154, v40, v41\n"
      "v_cvt_pk_bf16_f32 v155, v42, v43\n"
      "v_accvgpr_write_b32 a26, v154\n"
      "v_accvgpr_write_b32 a27, v155\n"
      "v_lshlrev_b32 v40, 16, v156\n"
      "v_and_b32 v41, s53, v156\n"
      "v_lshlrev_b32 v42, 16, v157\n"
      "v_and_b32 v43, s53, v157\n"
      "v_mfma_f32_16x16x32_bf16 v[4:7], a[176:179], v[144:147], v[4:7]\n"
      "v_mul_f32 v40, s40, v40\n"
      "v_mul_f32 v41, s40, v41\n"
      "v_mfma_f32_16x16x32_bf16 a[240:243], a[156:159], a[140:143], 0\n"
      "v_mul_f32 v42, s40, v42\n"
      "v_mul_f32 v43, s40, v43\n"
      "v_cvt_pk_bf16_f32 v156, v40, v41\n"
      "v_cvt_pk_bf16_f32 v157, v42, v43\n"
      "v_accvgpr_write_b32 a28, v156\n"
      "v_accvgpr_write_b32 a29, v157\n"
      "v_lshlrev_b32 v40, 16, v158\n"
      "v_and_b32 v41, s53, v158\n"
      "v_lshlrev_b32 v42, 16, v159\n"
      "v_and_b32 v43, s53, v159\n"
      "v_mfma_f32_16x16x32_bf16 v[4:7], a[180:183], v[148:151], v[4:7]\n"
      "v_mul_f32 v40, s40, v40\n"
      "v_mul_f32 v41, s40, v41\n"
      "v_mfma_f32_16x16x32_bf16 a[240:243], a[156:159], a[140:143], 0\n"
      "v_mul_f32 v42, s40, v42\n"
      "v_mul_f32 v43, s40, v43\n"
      "v_cvt_pk_bf16_f32 v158, v40, v41\n"
      "v_cvt_pk_bf16_f32 v159, v42, v43\n"
      "v_accvgpr_write_b32 a30, v158\n"
      "v_accvgpr_write_b32 a31, v159\n"
      "s_add_u32 m0, s93, 99200\n"
      "s_nop 0\n"
      "buffer_load_dwordx4 v108, s[28:31], 0 offen offset:128 nt lds\n"
      "s_add_u32 m0, s93, 103296\n"
      "s_nop 0\n"
      "buffer_load_dwordx4 v109, s[28:31], 0 offen offset:128 nt lds\n"
      "s_waitcnt vmcnt(8)\n"
      "ds_read_b128 v[168:171], v33 offset:1024\n"
      "ds_read_b128 v[172:175], v33 offset:1536\n"
      "s_waitcnt lgkmcnt(2)\n"
      "v_lshlrev_b32 v40, 16, v160\n"
      "v_and_b32 v41, s53, v160\n"
      "v_lshlrev_b32 v42, 16, v161\n"
      "v_and_b32 v43, s53, v161\n"
      "v_mfma_f32_16x16x32_bf16 v[0:3], a[152:155], v[152:155], v[0:3]\n"
      "v_mul_f32 v40, s40, v40\n"
      "v_mul_f32 v41, s40, v41\n"
      "v_mfma_f32_16x16x32_bf16 a[200:203], a[128:131], a[144:147], 0\n"
      "v_mul_f32 v42, s40, v42\n"
      "v_mul_f32 v43, s40, v43\n"
      "v_cvt_pk_bf16_f32 v160, v40, v41\n"
      "v_cvt_pk_bf16_f32 v161, v42, v43\n"
      "v_accvgpr_write_b32 a32, v160\n"
      "v_accvgpr_write_b32 a33, v161\n"
      "v_lshlrev_b32 v40, 16, v162\n"
      "v_and_b32 v41, s53, v162\n"
      "v_lshlrev_b32 v42, 16, v163\n"
      "v_and_b32 v43, s53, v163\n"
      "v_mfma_f32_16x16x32_bf16 v[0:3], a[156:159], v[156:159], v[0:3]\n"
      "v_mul_f32 v40, s40, v40\n"
      "v_mul_f32 v41, s40, v41\n"
      "v_mfma_f32_16x16x32_bf16 a[200:203], a[128:131], a[144:147], 0\n"
      "v_mul_f32 v42, s40, v42\n"
      "v_mul_f32 v43, s40, v43\n"
      "v_cvt_pk_bf16_f32 v162, v40, v41\n"
      "v_cvt_pk_bf16_f32 v163, v42, v43\n"
      "v_accvgpr_write_b32 a34, v162\n"
      "v_accvgpr_write_b32 a35, v163\n"
      "v_lshlrev_b32 v40, 16, v164\n"
      "v_and_b32 v41, s53, v164\n"
      "v_lshlrev_b32 v42, 16, v165\n"
      "v_and_b32 v43, s53, v165\n"
      "v_mfma_f32_16x16x32_bf16 v[4:7], a[184:187], v[152:155], v[4:7]\n"
      "v_mul_f32 v40, s40, v40\n"
      "v_mul_f32 v41, s40, v41\n"
      "v_mfma_f32_16x16x32_bf16 a[200:203], a[132:135], a[144:147], 0\n"
      "v_mul_f32 v42, s40, v42\n"
      "v_mul_f32 v43, s40, v43\n"
      "v_cvt_pk_bf16_f32 v164, v40, v41\n"
      "v_cvt_pk_bf16_f32 v165, v42, v43\n"
      "v_accvgpr_write_b32 a36, v164\n"
      "v_accvgpr_write_b32 a37, v165\n"
      "v_lshlrev_b32 v40, 16, v166\n"
      "v_and_b32 v41, s53, v166\n"
      "v_lshlrev_b32 v42, 16, v167\n"
      "v_and_b32 v43, s53, v167\n"
      "v_mfma_f32_16x16x32_bf16 v[4:7], a[188:191], v[156:159], v[4:7]\n"
      "v_mul_f32 v40, s40, v40\n"
      "v_mul_f32 v41, s40, v41\n"
      "v_mfma_f32_16x16x32_bf16 a[200:203], a[132:135], a[144:147], 0\n"
      "v_mul_f32 v42, s40, v42\n"
      "v_mul_f32 v43, s40, v43\n"
      "v_cvt_pk_bf16_f32 v166, v40, v41\n"
      "v_cvt_pk_bf16_f32 v167, v42, v43\n"
      "v_accvgpr_write_b32 a38, v166\n"
      "v_accvgpr_write_b32 a39, v167\n"
      "s_add_u32 m0, s93, 100096\n"
      "s_nop 0\n"
      "buffer_load_dwordx4 v108, s[28:31], 0 offen offset:256 nt lds\n"
      "s_add_u32 m0, s93, 104192\n"
      "s_nop 0\n"
      "buffer_load_dwordx4 v109, s[28:31], 0 offen offset:256 nt lds\n"
      "s_waitcnt vmcnt(8)\n"
      "ds_read_b128 v[176:179], v33 offset:2048\n"
      "ds_read_b128 v[180:183], v33 offset:2560\n"
      "s_waitcnt lgkmcnt(2)\n"
      "v_lshlrev_b32 v40, 16, v168\n"
      "v_and_b32 v41, s53, v168\n"
      "v_lshlrev_b32 v42, 16, v169\n"
      "v_and_b32 v43, s53, v169\n"
      "v_mfma_f32_16x16x32_bf16 v[8:11], a[128:131], v[160:163], 0\n"
      "v_mul_f32 v40, s40, v40\n"
      "v_mul_f32 v41, s40, v41\n"
      "v_mfma_f32_16x16x32_bf16 a[216:219], a[136:139], a[148:151], 0\n"
      "v_mul_f32 v42, s40, v42\n"
      "v_mul_f32 v43, s40, v43\n"
      "v_cvt_pk_bf16_f32 v168, v40, v41\n"
      "v_cvt_pk_bf16_f32 v169, v42, v43\n"
      "v_accvgpr_write_b32 a40, v168\n"
      "v_accvgpr_write_b32 a41, v169\n"
      "v_lshlrev_b32 v40, 16, v170\n"
      "v_and_b32 v41, s53, v170\n"
      "v_lshlrev_b32 v42, 16, v171\n"
      "v_and_b32 v43, s53, v171\n"
      "v_mfma_f32_16x16x32_bf16 v[8:11], a[132:135], v[164:167], v[8:11]\n"
      "v_mul_f32 v40, s40, v40\n"
      "v_mul_f32 v41, s40, v41\n"
      "v_mfma_f32_16x16x32_bf16 a[216:219], a[136:139], a[148:151], 0\n"
      "v_mul_f32 v42, s40, v42\n"
      "v_mul_f32 v43, s40, v43\n"
      "v_cvt_pk_bf16_f32 v170, v40, v41\n"
      "v_cvt_pk_bf16_f32 v171, v42, v43\n"
      "v_accvgpr_write_b32 a42, v170\n"
      "v_accvgpr_write_b32 a43, v171\n"
      "v_lshlrev_b32 v40, 16, v172\n"
      "v_and_b32 v41, s53, v172\n"
      "v_lshlrev_b32 v42, 16, v173\n"
      "v_and_b32 v43, s53, v173\n"
      "v_mfma_f32_16x16x32_bf16 v[12:15], a[160:163], v[160:163], 0\n"
      "v_mul_f32 v40, s40, v40\n"
      "v_mul_f32 v41, s40, v41\n"
      "v_mfma_f32_16x16x32_bf16 a[216:219], a[140:143], a[148:151], 0\n"
      "v_mul_f32 v42, s40, v42\n"
      "v_mul_f32 v43, s40, v43\n"
      "v_cvt_pk_bf16_f32 v172, v40, v41\n"
      "v_cvt_pk_bf16_f32 v173, v42, v43\n"
      "v_accvgpr_write_b32 a44, v172\n"
      "v_accvgpr_write_b32 a45, v173\n"
      "v_lshlrev_b32 v40, 16, v174\n"
      "v_and_b32 v41, s53, v174\n"
      "v_lshlrev_b32 v42, 16, v175\n"
      "v_and_b32 v43, s53, v175\n"
      "v_mfma_f32_16x16x32_bf16 v[12:15], a[164:167], v[164:167], v[12:15]\n"
      "v_mul_f32 v40, s40, v40\n"
      "v_mul_f32 v41, s40, v41\n"
      "v_mfma_f32_16x16x32_bf16 a[216:219], a[140:143], a[148:151], 0\n"
      "v_mul_f32 v42, s40, v42\n"
      "v_mul_f32 v43, s40, v43\n"
      "v_cvt_pk_bf16_f32 v174, v40, v41\n"
      "v_cvt_pk_bf16_f32 v175, v42, v43\n"
      "v_accvgpr_write_b32 a46, v174\n"
      "v_accvgpr_write_b32 a47, v175\n"
      "v_max3_f32 v124, v0, v1, v2\n"
      "v_max3_f32 v125, v3, v4, v5\n"
      "v_max3_f32 v124, v124, v6, v7\n"
      "v_max_f32 v124, v124, v125\n"
      "v_mov_b32 v125, v124\n"
      "s_nop 1\n"
      "v_permlane32_swap_b32 v124, v125\n"
      "v_max_f32 v124, v124, v125\n"
      "v_mov_b32 v125, v124\n"
      "s_nop 1\n"
      "v_permlane16_swap_b32 v124, v125\n"
      "v_max_f32 v124, v124, v125\n"
      "v_sub_f32 v0, v0, v124\n"
      "v_sub_f32 v1, v1, v124\n"
      "v_sub_f32 v2, v2, v124\n"
      "v_sub_f32 v3, v3, v124\n"
      "v_sub_f32 v4, v4, v124\n"
      "v_sub_f32 v5, v5, v124\n"
      "v_sub_f32 v6, v6, v124\n"
      "v_sub_f32 v7, v7, v124\n"
      "v_sub_f32 v96, 0, v124\n"
      "v_sub_f32 v97, 0, v124\n"
      "v_sub_f32 v98, 0, v124\n"
      "v_sub_f32 v99, 0, v124\n"
      "v_exp_f32 v0, v0\n"
      "v_exp_f32 v1, v1\n"
      "v_exp_f32 v2, v2\n"
      "v_exp_f32 v3, v3\n"
      "v_exp_f32 v4, v4\n"
      "v_exp_f32 v5, v5\n"
      "v_exp_f32 v6, v6\n"
      "v_exp_f32 v7, v7\n"
      "v_add_f32 v112, v0, v1\n"
      "v_add_f32 v112, v112, v2\n"
      "v_add_f32 v112, v112, v3\n"
      "v_add_f32 v112, v112, v4\n"
      "v_add_f32 v112, v112, v5\n"
      "v_add_f32 v112, v112, v6\n"
      "v_add_f32 v112, v112, v7\n"
      "v_cvt_pk_bf16_f32 v0, v0, v1\n"
      "v_cvt_pk_bf16_f32 v1, v2, v3\n"
      "v_cvt_pk_bf16_f32 v2, v4, v5\n"
      "v_cvt_pk_bf16_f32 v3, v6, v7\n"
      "s_add_u32 m0, s93, 100992\n"
      "s_nop 0\n"
      "buffer_load_dwordx4 v108, s[28:31], 0 offen offset:384 nt lds\n"
      "v_add_u32 v108, 8192, v108\n"
      "s_add_u32 m0, s93, 105088\n"
      "s_nop 0\n"
      "buffer_load_dwordx4 v109, s[28:31], 0 offen offset:384 nt lds\n"
      "v_add_u32 v109, 8192, v109\n"
      "s_waitcnt vmcnt(8)\n"
      "ds_read_b128 v[184:187], v33 offset:3072\n"
      "ds_read_b128 v[188:191], v33 offset:3584\n"
      "s_waitcnt lgkmcnt(2)\n"
      "v_lshlrev_b32 v40, 16, v176\n"
      "v_and_b32 v41, s53, v176\n"
      "v_lshlrev_b32 v42, 16, v177\n"
      "v_and_b32 v43, s53, v177\n"
      "v_mfma_f32_16x16x32_bf16 v[8:11], a[136:139], v[168:171], v[8:11]\n"
      "v_mul_f32 v40, s40, v40\n"
      "v_mul_f32 v41, s40, v41\n"
      "v_mfma_f32_16x16x32_bf16 a[232:235], a[144:147], a[152:155], 0\n"
      "v_mul_f32 v42, s40, v42\n"
      "v_mul_f32 v43, s40, v43\n"
      "v_cvt_pk_bf16_f32 v176, v40, v41\n"
      "v_cvt_pk_bf16_f32 v177, v42, v43\n"
      "v_accvgpr_write_b32 a48, v176\n"
      "v_accvgpr_write_b32 a49, v177\n"
      "v_lshlrev_b32 v40, 16, v178\n"
      "v_and_b32 v41, s53, v178\n"
      "v_lshlrev_b32 v42, 16, v179\n"
      "v_and_b32 v43, s53, v179\n"
      "v_mfma_f32_16x16x32_bf16 v[8:11], a[140:143], v[172:175], v[8:11]\n"
      "v_mul_f32 v40, s40, v40\n"
      "v_mul_f32 v41, s40, v41\n"
      "v_mfma_f32_16x16x32_bf16 a[232:235], a[144:147], a[152:155], 0\n"
      "v_mul_f32 v42, s40, v42\n"
      "v_mul_f32 v43, s40, v43\n"
      "v_cvt_pk_bf16_f32 v178, v40, v41\n"
      "v_cvt_pk_bf16_f32 v179, v42, v43\n"
      "v_accvgpr_write_b32 a50, v178\n"
      "v_accvgpr_write_b32 a51, v179\n"
      "v_lshlrev_b32 v40, 16, v180\n"
      "v_and_b32 v41, s53, v180\n"
      "v_lshlrev_b32 v42, 16, v181\n"
      "v_and_b32 v43, s53, v181\n"
      "v_mfma_f32_16x16x32_bf16 v[12:15], a[168:171], v[168:171], v[12:15]\n"
      "v_mul_f32 v40, s40, v40\n"
      "v_mul_f32 v41, s40, v41\n"
      "v_mfma_f32_16x16x32_bf16 a[232:235], a[148:151], a[152:155], 0\n"
      "v_mul_f32 v42, s40, v42\n"
      "v_mul_f32 v43, s40, v43\n"
      "v_cvt_pk_bf16_f32 v180, v40, v41\n"
      "v_cvt_pk_bf16_f32 v181, v42, v43\n"
      "v_accvgpr_write_b32 a52, v180\n"
      "v_accvgpr_write_b32 a53, v181\n"
      "v_lshlrev_b32 v40, 16, v182\n"
      "v_and_b32 v41, s53, v182\n"
      "v_lshlrev_b32 v42, 16, v183\n"
      "v_and_b32 v43, s53, v183\n"
      "v_mfma_f32_16x16x32_bf16 v[12:15], a[172:175], v[172:175], v[12:15]\n"
      "v_mul_f32 v40, s40, v40\n"
      "v_mul_f32 v41, s40, v41\n"
      "v_mfma_f32_16x16x32_bf16 a[232:235], a[148:151], a[152:155], 0\n"
      "v_mul_f32 v42, s40, v42\n"
      "v_mul_f32 v43, s40, v43\n"
      "v_cvt_pk_bf16_f32 v182, v40, v41\n"
      "v_cvt_pk_bf16_f32 v183, v42, v43\n"
      "v_accvgpr_write_b32 a54, v182\n"
      "v_accvgpr_write_b32 a55, v183\n"
      "s_add_u32 m0, s93, 131072\n"
      "s_nop 0\n"
      "buffer_load_dwordx4 v108, s[28:31], 0 offen offset:0 nt lds\n"
      "s_add_u32 m0, s93, 135168\n"
      "s_nop 0\n"
      "buffer_load_dwordx4 v109, s[28:31], 0 offen offset:0 nt lds\n"
      "s_waitcnt vmcnt(8)\n"
      "ds_read_b128 v[192:195], v34 offset:0\n"
      "ds_read_b128 v[196:199], v34 offset:512\n"
      "s_waitcnt lgkmcnt(2)\n"
      "v_lshlrev_b32 v40, 16, v184\n"
      "v_and_b32 v41, s53, v184\n"
      "v_lshlrev_b32 v42, 16, v185\n"
      "v_and_b32 v43, s53, v185\n"
      "v_mfma_f32_16x16x32_bf16 v[8:11], a[144:147], v[176:179], v[8:11]\n"
      "v_mul_f32 v40, s40, v40\n"
      "v_mul_f32 v41, s40, v41\n"
      "v_mfma_f32_16x16x32_bf16 a[192:195], a[152:155], a[156:159], 0\n"
      "v_mul_f32 v42, s40, v42\n"
      "v_mul_f32 v43, s40, v43\n"
      "v_cvt_pk_bf16_f32 v184, v40, v41\n"
      "v_cvt_pk_bf16_f32 v185, v42, v43\n"
      "v_accvgpr_write_b32 a56, v184\n"
      "v_accvgpr_write_b32 a57, v185\n"
      "v_lshlrev_b32 v40, 16, v186\n"
      "v_and_b32 v41, s53, v186\n"
      "v_lshlrev_b32 v42, 16, v187\n"
      "v_and_b32 v43, s53, v187\n"
      "v_mfma_f32_16x16x32_bf16 v[8:11], a[148:151], v[180:183], v[8:11]\n"
      "v_mul_f32 v40, s40, v40\n"
      "v_mul_f32 v41, s40, v41\n"
      "v_mfma_f32_16x16x32_bf16 a[192:195], a[152:155], a[156:159], 0\n"
      "v_mul_f32 v42, s40, v42\n"
      "v_mul_f32 v43, s40, v43\n"
      "v_cvt_pk_bf16_f32 v186, v40, v41\n"
      "v_cvt_pk_bf16_f32 v187, v42, v43\n"
      "v_accvgpr_write_b32 a58, v186\n"
      "v_accvgpr_write_b32 a59, v187\n"
      "v_lshlrev_b32 v40, 16, v188\n"
      "v_and_b32 v41, s53, v188\n"
      "v_lshlrev_b32 v42, 16, v189\n"
      "v_and_b32 v43, s53, v189\n"
      "v_mfma_f32_16x16x32_bf16 v[12:15], a[176:179], v[176:179], v[12:15]\n"
      "v_mul_f32 v40, s40, v40\n"
      "v_mul_f32 v41, s40, v41\n"
      "v_mfma_f32_16x16x32_bf16 a[192:195], a[156:159], a[156:159], 0\n"
      "v_mul_f32 v42, s40, v42\n"
      "v_mul_f32 v43, s40, v43\n"
      "v_cvt_pk_bf16_f32 v188, v40, v41\n"
      "v_cvt_pk_bf16_f32 v189, v42, v43\n"
      "v_accvgpr_write_b32 a60, v188\n"
      "v_accvgpr_write_b32 a61, v189\n"
      "v_lshlrev_b32 v40, 16, v190\n"
      "v_and_b32 v41, s53, v190\n"
      "v_lshlrev_b32 v42, 16, v191\n"
      "v_and_b32 v43, s53, v191\n"
      "v_mfma_f32_16x16x32_bf16 v[12:15], a[180:183], v[180:183], v[12:15]\n"
      "v_mul_f32 v40, s40, v40\n"
      "v_mul_f32 v41, s40, v41\n"
      "v_mfma_f32_16x16x32_bf16 a[192:195], a[156:159], a[156:159], 0\n"
      "v_mul_f32 v42, s40, v42\n"
      "v_mul_f32 v43, s40, v43\n"
      "v_cvt_pk_bf16_f32 v190, v40, v41\n"
      "v_cvt_pk_bf16_f32 v191, v42, v43\n"
      "v_accvgpr_write_b32 a62, v190\n"
      "v_accvgpr_write_b32 a63, v191\n"
      "s_add_u32 m0, s93, 131968\n"
      "s_nop 0\n"
      "buffer_load_dwordx4 v108, s[28:31], 0 offen offset:128 nt lds\n"
      "s_add_u32 m0, s93, 136064\n"
      "s_nop 0\n"
      "buffer_load_dwordx4 v109, s[28:31], 0 offen offset:128 nt lds\n"
      "s_waitcnt vmcnt(8)\n"
      "ds_read_b128 v[200:203], v34 offset:1024\n"
      "ds_read_b128 v[204:207], v34 offset:1536\n"
      "s_waitcnt lgkmcnt(2)\n"
      "s_barrier\n"
      "v_lshlrev_b32 v40, 16, v192\n"
      "v_and_b32 v41, s53, v192\n"
      "v_lshlrev_b32 v42, 16, v193\n"
      "v_and_b32 v43, s53, v193\n"
      "v_mfma_f32_16x16x32_bf16 v[8:11], a[152:155], v[184:187], v[8:11]\n"
      "v_mul_f32 v40, s40, v40\n"
      "v_mul_f32 v41, s40, v41\n"
      "v_mfma_f32_16x16x32_bf16 a[208:211], a[128:131], a[160:163], 0\n"
      "v_mul_f32 v42, s40, v42\n"
      "v_mul_f32 v43, s40, v43\n"
      "v_cvt_pk_bf16_f32 v192, v40, v41\n"
      "v_cvt_pk_bf16_f32 v193, v42, v43\n"
      "v_accvgpr_write_b32 a64, v192\n"
      "v_accvgpr_write_b32 a65, v193\n"
      "v_lshlrev_b32 v40, 16, v194\n"
      "v_and_b32 v41, s53, v194\n"
      "v_lshlrev_b32 v42, 16, v195\n"
      "v_and_b32 v43, s53, v195\n"
      "v_mfma_f32_16x16x32_bf16 v[8:11], a[156:159], v[188:191], v[8:11]\n"
      "v_mul_f32 v40, s40, v40\n"
      "v_mul_f32 v41, s40, v41\n"
      "v_mfma_f32_16x16x32_bf16 a[208:211], a[128:131], a[160:163], 0\n"
      "v_mul_f32 v42, s40, v42\n"
      "v_mul_f32 v43, s40, v43\n"
      "v_cvt_pk_bf16_f32 v194, v40, v41\n"
      "v_cvt_pk_bf16_f32 v195, v42, v43\n"
      "v_accvgpr_write_b32 a66, v194\n"
      "v_accvgpr_write_b32 a67, v195\n"
      "v_lshlrev_b32 v40, 16, v196\n"
      "v_and_b32 v41, s53, v196\n"
      "v_lshlrev_b32 v42, 16, v197\n"
      "v_and_b32 v43, s53, v197\n"
      "v_mfma_f32_16x16x32_bf16 v[12:15], a[184:187], v[184:187], v[12:15]\n"
      "v_mul_f32 v40, s40, v40\n"
      "v_mul_f32 v41, s40, v41\n"
      "v_mfma_f32_16x16x32_bf16 a[208:211], a[132:135], a[160:163], 0\n"
      "v_mul_f32 v42, s40, v42\n"
      "v_mul_f32 v43, s40, v43\n"
      "v_cvt_pk_bf16_f32 v196, v40, v41\n"
      "v_cvt_pk_bf16_f32 v197, v42, v43\n"
      "v_accvgpr_write_b32 a68, v196\n"
      "v_accvgpr_write_b32 a69, v197\n"
      "v_lshlrev_b32 v40, 16, v198\n"
      "v_and_b32 v41, s53, v198\n"
      "v_lshlrev_b32 v42, 16, v199\n"
      "v_and_b32 v43, s53, v199\n"
      "v_mfma_f32_16x16x32_bf16 v[12:15], a[188:191], v[188:191], v[12:15]\n"
      "v_mul_f32 v40, s40, v40\n"
      "v_mul_f32 v41, s40, v41\n"
      "v_mfma_f32_16x16x32_bf16 a[208:211], a[132:135], a[160:163], 0\n"
      "v_mul_f32 v42, s40, v42\n"
      "v_mul_f32 v43, s40, v43\n"
      "v_cvt_pk_bf16_f32 v198, v40, v41\n"
      "v_cvt_pk_bf16_f32 v199, v42, v43\n"
      "v_accvgpr_write_b32 a70, v198\n"
      "v_accvgpr_write_b32 a71, v199\n"
      "s_add_u32 m0, s93, 132864\n"
      "s_nop 0\n"
      "buffer_load_dwordx4 v108, s[28:31], 0 offen offset:256 nt lds\n"
      "s_add_u32 m0, s93, 136960\n"
      "s_nop 0\n"
      "buffer_load_dwordx4 v109, s[28:31], 0 offen offset:256 nt lds\n"
      "s_waitcnt vmcnt(8)\n"
      "ds_read_b128 v[208:211], v34 offset:2048\n"
      "ds_read_b128 v[212:215], v34 offset:2560\n"
      "s_waitcnt lgkmcnt(2)\n"
      "v_lshlrev_b32 v40, 16, v200\n"
      "v_and_b32 v41, s53, v200\n"
      "v_lshlrev_b32 v42, 16, v201\n"
      "v_and_b32 v43, s53, v201\n"
      "v_mfma_f32_16x16x32_bf16 v[16:19], a[128:131], v[192:195], 0\n"
      "v_mul_f32 v40, s40, v40\n"
      "v_mul_f32 v41, s40, v41\n"
      "v_mfma_f32_16x16x32_bf16 a[224:227], a[136:139], a[164:167], 0\n"
      "v_mul_f32 v42, s40, v42\n"
      "v_mul_f32 v43, s40, v43\n"
      "v_cvt_pk_bf16_f32 v200, v40, v41\n"
      "v_cvt_pk_bf16_f32 v201, v42, v43\n"
      "v_accvgpr_write_b32 a72, v200\n"
      "v_accvgpr_write_b32 a73, v201\n"
      "v_lshlrev_b32 v40, 16, v202\n"
      "v_and_b32 v41, s53, v202\n"
      "v_lshlrev_b32 v42, 16, v203\n"
      "v_and_b32 v43, s53, v203\n"
      "v_mfma_f32_16x16x32_bf16 v[16:19], a[132:135], v[196:199], v[16:19]\n"
      "v_mul_f32 v40, s40, v40\n"
      "v_mul_f32 v41, s40, v41\n"
      "v_mfma_f32_16x16x32_bf16 a[224:227], a[136:139], a[164:167], 0\n"
      "v_mul_f32 v42, s40, v42\n"
      "v_mul_f32 v43, s40, v43\n"
      "v_cvt_pk_bf16_f32 v202, v40, v41\n"
      "v_cvt_pk_bf16_f32 v203, v42, v43\n"
      "v_accvgpr_write_b32 a74, v202\n"
      "v_accvgpr_write_b32 a75, v203\n"
      "v_lshlrev_b32 v40, 16, v204\n"
      "v_and_b32 v41, s53, v204\n"
      "v_lshlrev_b32 v42, 16, v205\n"
      "v_and_b32 v43, s53, v205\n"
      "v_mfma_f32_16x16x32_bf16 v[20:23], a[160:163], v[192:195], 0\n"
      "v_mul_f32 v40, s40, v40\n"
      "v_mul_f32 v41, s40, v41\n"
      "v_mfma_f32_16x16x32_bf16 a[224:227], a[140:143], a[164:167], 0\n"
      "v_mul_f32 v42, s40, v42\n"
      "v_mul_f32 v43, s40, v43\n"
      "v_cvt_pk_bf16_f32 v204, v40, v41\n"
      "v_cvt_pk_bf16_f32 v205, v42, v43\n"
      "v_accvgpr_write_b32 a76, v204\n"
      "v_accvgpr_write_b32 a77, v205\n"
      "v_lshlrev_b32 v40, 16, v206\n"
      "v_and_b32 v41, s53, v206\n"
      "v_lshlrev_b32 v42, 16, v207\n"
      "v_and_b32 v43, s53, v207\n"
      "v_mfma_f32_16x16x32_bf16 v[20:23], a[164:167], v[196:199], v[20:23]\n"
      "v_mul_f32 v40, s40, v40\n"
      "v_mul_f32 v41, s40, v41\n"
      "v_mfma_f32_16x16x32_bf16 a[224:227], a[140:143], a[164:167], 0\n"
      "v_mul_f32 v42, s40, v42\n"
      "v_mul_f32 v43, s40, v43\n"
      "v_cvt_pk_bf16_f32 v206, v40, v41\n"
      "v_cvt_pk_bf16_f32 v207, v42, v43\n"
      "v_accvgpr_write_b32 a78, v206\n"
      "v_accvgpr_write_b32 a79, v207\n"
      "v_max3_f32 v124, v8, v9, v10\n"
      "v_max3_f32 v125, v11, v12, v13\n"
      "v_max3_f32 v124, v124, v14, v15\n"
      "v_max_f32 v124, v124, v125\n"
      "v_mov_b32 v125, v124\n"
      "s_nop 1\n"
      "v_permlane32_swap_b32 v124, v125\n"
      "v_max_f32 v124, v124, v125\n"
      "v_mov_b32 v125, v124\n"
      "s_nop 1\n"
      "v_permlane16_swap_b32 v124, v125\n"
      "v_max_f32 v124, v124, v125\n"
      "v_sub_f32 v8, v8, v124\n"
      "v_sub_f32 v9, v9, v124\n"
      "v_sub_f32 v10, v10, v124\n"
      "v_sub_f32 v11, v11, v124\n"
      "v_sub_f32 v12, v12, v124\n"
      "v_sub_f32 v13, v13, v124\n"
      "v_sub_f32 v14, v14, v124\n"
      "v_sub_f32 v15, v15, v124\n"
      "v_sub_f32 v100, 0, v124\n"
      "v_sub_f32 v101, 0, v124\n"
      "v_sub_f32 v102, 0, v124\n"
      "v_sub_f32 v103, 0, v124\n"
      "v_exp_f32 v8, v8\n"
      "v_exp_f32 v9, v9\n"
      "v_exp_f32 v10, v10\n"
      "v_exp_f32 v11, v11\n"
      "v_exp_f32 v12, v12\n"
      "v_exp_f32 v13, v13\n"
      "v_exp_f32 v14, v14\n"
      "v_exp_f32 v15, v15\n"
      "v_add_f32 v113, v8, v9\n"
      "v_add_f32 v113, v113, v10\n"
      "v_add_f32 v113, v113, v11\n"
      "v_add_f32 v113, v113, v12\n"
      "v_add_f32 v113, v113, v13\n"
      "v_add_f32 v113, v113, v14\n"
      "v_add_f32 v113, v113, v15\n"
      "v_cvt_pk_bf16_f32 v8, v8, v9\n"
      "v_cvt_pk_bf16_f32 v9, v10, v11\n"
      "v_cvt_pk_bf16_f32 v10, v12, v13\n"
      "v_cvt_pk_bf16_f32 v11, v14, v15\n"
      "s_add_u32 m0, s93, 133760\n"
      "s_nop 0\n"
      "buffer_load_dwordx4 v108, s[28:31], 0 offen offset:384 nt lds\n"
      "s_add_u32 m0, s93, 137856\n"
      "s_nop 0\n"
      "buffer_load_dwordx4 v109, s[28:31], 0 offen offset:384 nt lds\n"
      "s_waitcnt vmcnt(8)\n"
      "ds_read_b128 v[216:219], v34 offset:3072\n"
      "ds_read_b128 v[220:223], v34 offset:3584\n"
      "s_waitcnt lgkmcnt(2)\n"
      "v_lshlrev_b32 v40, 16, v208\n"
      "v_and_b32 v41, s53, v208\n"
      "v_lshlrev_b32 v42, 16, v209\n"
      "v_and_b32 v43, s53, v209\n"
      "v_mfma_f32_16x16x32_bf16 v[16:19], a[136:139], v[200:203], v[16:19]\n"
      "v_mul_f32 v40, s40, v40\n"
      "v_mul_f32 v41, s40, v41\n"
      "v_mfma_f32_16x16x32_bf16 a[240:243], a[144:147], a[168:171], 0\n"
      "v_mul_f32 v42, s40, v42\n"
      "v_mul_f32 v43, s40, v43\n"
      "v_cvt_pk_bf16_f32 v208, v40, v41\n"
      "v_cvt_pk_bf16_f32 v209, v42, v43\n"
      "v_accvgpr_write_b32 a80, v208\n"
      "v_accvgpr_write_b32 a81, v209\n"
      "v_lshlrev_b32 v40, 16, v210\n"
      "v_and_b32 v41, s53, v210\n"
      "v_lshlrev_b32 v42, 16, v211\n"
      "v_and_b32 v43, s53, v211\n"
      "v_mfma_f32_16x16x32_bf16 v[16:19], a[140:143], v[204:207], v[16:19]\n"
      "v_mul_f32 v40, s40, v40\n"
      "v_mul_f32 v41, s40, v41\n"
      "v_mfma_f32_16x16x32_bf16 a[240:243], a[144:147], a[168:171], 0\n"
      "v_mul_f32 v42, s40, v42\n"
      "v_mul_f32 v43, s40, v43\n"
      "v_cvt_pk_bf16_f32 v210, v40, v41\n"
      "v_cvt_pk_bf16_f32 v211, v42, v43\n"
      "v_accvgpr_write_b32 a82, v210\n"
      "v_accvgpr_write_b32 a83, v211\n"
      "v_lshlrev_b32 v40, 16, v212\n"
      "v_and_b32 v41, s53, v212\n"
      "v_lshlrev_b32 v42, 16, v213\n"
      "v_and_b32 v43, s53, v213\n"
      "v_mfma_f32_16x16x32_bf16 v[20:23], a[168:171], v[200:203], v[20:23]\n"
      "v_mul_f32 v40, s40, v40\n"
      "v_mul_f32 v41, s40, v41\n"
      "v_mfma_f32_16x16x32_bf16 a[240:243], a[148:151], a[168:171], 0\n"
      "v_mul_f32 v42, s40, v42\n"
      "v_mul_f32 v43, s40, v43\n"
      "v_cvt_pk_bf16_f32 v212, v40, v41\n"
      "v_cvt_pk_bf16_f32 v213, v42, v43\n"
      "v_accvgpr_write_b32 a84, v212\n"
      "v_accvgpr_write_b32 a85, v213\n"
      "v_lshlrev_b32 v40, 16, v214\n"
      "v_and_b32 v41, s53, v214\n"
      "v_lshlrev_b32 v42, 16, v215\n"
      "v_and_b32 v43, s53, v215\n"
      "v_mfma_f32_16x16x32_bf16 v[20:23], a[172:175], v[204:207], v[20:23]\n"
      "v_mul_f32 v40, s40, v40\n"
      "v_mul_f32 v41, s40, v41\n"
      "v_mfma_f32_16x16x32_bf16 a[240:243], a[148:151], a[168:171], 0\n"
      "v_mul_f32 v42, s40, v42\n"
      "v_mul_f32 v43, s40, v43\n"
      "v_cvt_pk_bf16_f32 v214, v40, v41\n"
      "v_cvt_pk_bf16_f32 v215, v42, v43\n"
      "v_accvgpr_write_b32 a86, v214\n"
      "v_accvgpr_write_b32 a87, v215\n"
      "s_mov_b32 s52, 16384\n"
      "s_add_u32 m0, s92, 16384\n"
      "s_nop 0\n"
      "buffer_load_dwordx4 v119, s[20:23], s52 offen offset:0 nt lds\n"
      "s_add_u32 m0, s92, 17280\n"
      "s_nop 0\n"
      "buffer_load_dwordx4 v119, s[20:23], s52 offen offset:128 nt lds\n"
      "s_add_u32 m0, s92, 18176\n"
      "s_nop 0\n"
      "buffer_load_dwordx4 v119, s[20:23], s52 offen offset:256 nt lds\n"
      "s_waitcnt vmcnt(9)\n"
      "ds_read_b128 v[224:227], v35 offset:0\n"
      "ds_read_b128 v[228:231], v35 offset:512\n"
      "s_waitcnt lgkmcnt(2)\n"
      "v_lshlrev_b32 v40, 16, v216\n"
      "v_and_b32 v41, s53, v216\n"
      "v_lshlrev_b32 v42, 16, v217\n"
      "v_and_b32 v43, s53, v217\n"
      "v_mfma_f32_16x16x32_bf16 v[16:19], a[144:147], v[208:211], v[16:19]\n"
      "v_mul_f32 v40, s40, v40\n"
      "v_mul_f32 v41, s40, v41\n"
      "v_mfma_f32_16x16x32_bf16 a[200:203], a[152:155], a[172:175], 0\n"
      "v_mul_f32 v42, s40, v42\n"
      "v_mul_f32 v43, s40, v43\n"
      "v_cvt_pk_bf16_f32 v216, v40, v41\n"
      "v_cvt_pk_bf16_f32 v217, v42, v43\n"
      "v_accvgpr_write_b32 a88, v216\n"
      "v_accvgpr_write_b32 a89, v217\n"
      "v_lshlrev_b32 v40, 16, v218\n"
      "v_and_b32 v41, s53, v218\n"
      "v_lshlrev_b32 v42, 16, v219\n"
      "v_and_b32 v43, s53, v219\n"
      "v_mfma_f32_16x16x32_bf16 v[16:19], a[148:151], v[212:215], v[16:19]\n"
      "v_mul_f32 v40, s40, v40\n"
      "v_mul_f32 v41, s40, v41\n"
      "v_mfma_f32_16x16x32_bf16 a[200:203], a[152:155], a[172:175], 0\n"
      "v_mul_f32 v42, s40, v42\n"
      "v_mul_f32 v43, s40, v43\n"
      "v_cvt_pk_bf16_f32 v218, v40, v41\n"
      "v_cvt_pk_bf16_f32 v219, v42, v43\n"
      "v_accvgpr_write_b32 a90, v218\n"
      "v_accvgpr_write_b32 a91, v219\n"
      "v_lshlrev_b32 v40, 16, v220\n"
      "v_and_b32 v41, s53, v220\n"
      "v_lshlrev_b32 v42, 16, v221\n"
      "v_and_b32 v43, s53, v221\n"
      "v_mfma_f32_16x16x32_bf16 v[20:23], a[176:179], v[208:211], v[20:23]\n"
      "v_mul_f32 v40, s40, v40\n"
      "v_mul_f32 v41, s40, v41\n"
      "v_mfma_f32_16x16x32_bf16 a[200:203], a[156:159], a[172:175], 0\n"
      "v_mul_f32 v42, s40, v42\n"
      "v_mul_f32 v43, s40, v43\n"
      "v_cvt_pk_bf16_f32 v220, v40, v41\n"
      "v_cvt_pk_bf16_f32 v221, v42, v43\n"
      "v_accvgpr_write_b32 a92, v220\n"
      "v_accvgpr_write_b32 a93, v221\n"
      "v_lshlrev_b32 v40, 16, v222\n"
      "v_and_b32 v41, s53, v222\n"
      "v_lshlrev_b32 v42, 16, v223\n"
      "v_and_b32 v43, s53, v223\n"
      "v_mfma_f32_16x16x32_bf16 v[20:23], a[180:183], v[212:215], v[20:23]\n"
      "v_mul_f32 v40, s40, v40\n"
      "v_mul_f32 v41, s40, v41\n"
      "v_mfma_f32_16x16x32_bf16 a[200:203], a[156:159], a[172:175], 0\n"
      "v_mul_f32 v42, s40, v42\n"
      "v_mul_f32 v43, s40, v43\n"
      "v_cvt_pk_bf16_f32 v222, v40, v41\n"
      "v_cvt_pk_bf16_f32 v223, v42, v43\n"
      "v_accvgpr_write_b32 a94, v222\n"
      "v_accvgpr_write_b32 a95, v223\n"
      "s_add_u32 m0, s92, 19072\n"
      "s_nop 0\n"
      "buffer_load_dwordx4 v119, s[20:23], s52 offen offset:384 nt lds\n"
      "global_load_dwordx4 a[252:255], v36, s[50:51] offset:0\n"
      "global_load_dwordx4 a[252:255], v36, s[50:51] offset:1024\n"
      "s_waitcnt vmcnt(10)\n"
      "ds_read_b128 v[232:235], v35 offset:1024\n"
      "ds_read_b128 v[236:239], v35 offset:1536\n"
      "s_waitcnt lgkmcnt(2)\n"
      "v_lshlrev_b32 v40, 16, v224\n"
      "v_and_b32 v41, s53, v224\n"
      "v_lshlrev_b32 v42, 16, v225\n"
      "v_and_b32 v43, s53, v225\n"
      "v_mfma_f32_16x16x32_bf16 v[16:19], a[152:155], v[216:219], v[16:19]\n"
      "v_mul_f32 v40, s40, v40\n"
      "v_mul_f32 v41, s40, v41\n"
      "v_mfma_f32_16x16x32_bf16 a[216:219], a[128:131], a[176:179], 0\n"
      "v_mul_f32 v42, s40, v42\n"
      "v_mul_f32 v43, s40, v43\n"
      "v_cvt_pk_bf16_f32 v224, v40, v41\n"
      "v_cvt_pk_bf16_f32 v225, v42, v43\n"
      "v_lshlrev_b32 v40, 16, v226\n"
      "v_and_b32 v41, s53, v226\n"
      "v_lshlrev_b32 v42, 16, v227\n"
      "v_and_b32 v43, s53, v227\n"
      "v_mfma_f32_16x16x32_bf16 v[16:19], a[156:159], v[220:223], v[16:19]\n"
      "v_mul_f32 v40, s40, v40\n"
      "v_mul_f32 v41, s40, v41\n"
      "v_mfma_f32_16x16x32_bf16 a[216:219], a[128:131], a[176:179], 0\n"
      "v_mul_f32 v42, s40, v42\n"
      "v_mul_f32 v43, s40, v43\n"
      "v_cvt_pk_bf16_f32 v226, v40, v41\n"
      "v_cvt_pk_bf16_f32 v227, v42, v43\n"
      "v_lshlrev_b32 v40, 16, v228\n"
      "v_and_b32 v41, s53, v228\n"
      "v_lshlrev_b32 v42, 16, v229\n"
      "v_and_b32 v43, s53, v229\n"
      "v_mfma_f32_16x16x32_bf16 v[20:23], a[184:187], v[216:219], v[20:23]\n"
      "v_mul_f32 v40, s40, v40\n"
      "v_mul_f32 v41, s40, v41\n"
      "v_mfma_f32_16x16x32_bf16 a[216:219], a[132:135], a[176:179], 0\n"
      "v_mul_f32 v42, s40, v42\n"
      "v_mul_f32 v43, s40, v43\n"
      "v_cvt_pk_bf16_f32 v228, v40, v41\n"
      "v_cvt_pk_bf16_f32 v229, v42, v43\n"
      "v_lshlrev_b32 v40, 16, v230\n"
      "v_and_b32 v41, s53, v230\n"
      "v_lshlrev_b32 v42, 16, v231\n"
      "v_and_b32 v43, s53, v231\n"
      "v_mfma_f32_16x16x32_bf16 v[20:23], a[188:191], v[220:223], v[20:23]\n"
      "v_mul_f32 v40, s40, v40\n"
      "v_mul_f32 v41, s40, v41\n"
      "v_mfma_f32_16x16x32_bf16 a[216:219], a[132:135], a[176:179], 0\n"
      "v_mul_f32 v42, s40, v42\n"
      "v_mul_f32 v43, s40, v43\n"
      "v_cvt_pk_bf16_f32 v230, v40, v41\n"
      "v_cvt_pk_bf16_f32 v231, v42, v43\n"
      "global_load_dwordx4 a[252:255], v36, s[50:51] offset:2048\n"
      "global_load_dwordx4 a[252:255], v36, s[50:51] offset:3072\n"
      "s_waitcnt vmcnt(10)\n"
      "ds_read_b128 v[240:243], v35 offset:2048\n"
      "ds_read_b128 v[244:247], v35 offset:2560\n"
      "s_waitcnt lgkmcnt(2)\n"
      "v_lshlrev_b32 v40, 16, v232\n"
      "v_and_b32 v41, s53, v232\n"
      "v_lshlrev_b32 v42, 16, v233\n"
      "v_and_b32 v43, s53, v233\n"
      "v_mfma_f32_16x16x32_bf16 v[24:27], a[128:131], v[224:227], 0\n"
      "v_mul_f32 v40, s40, v40\n"
      "v_mul_f32 v41, s40, v41\n"
      "v_mfma_f32_16x16x32_bf16 a[232:235], a[136:139], a[180:183], 0\n"
      "v_mul_f32 v42, s40, v42\n"
      "v_mul_f32 v43, s40, v43\n"
      "v_cvt_pk_bf16_f32 v232, v40, v41\n"
      "v_cvt_pk_bf16_f32 v233, v42, v43\n"
      "v_lshlrev_b32 v40, 16, v234\n"
      "v_and_b32 v41, s53, v234\n"
      "v_lshlrev_b32 v42, 16, v235\n"
      "v_and_b32 v43, s53, v235\n"
      "v_mfma_f32_16x16x32_bf16 v[24:27], a[132:135], v[228:231], v[24:27]\n"
      "v_mul_f32 v40, s40, v40\n"
      "v_mul_f32 v41, s40, v41\n"
      "v_mfma_f32_16x16x32_bf16 a[232:235], a[136:139], a[180:183], 0\n"
      "v_mul_f32 v42, s40, v42\n"
      "v_mul_f32 v43, s40, v43\n"
      "v_cvt_pk_bf16_f32 v234, v40, v41\n"
      "v_cvt_pk_bf16_f32 v235, v42, v43\n"
      "v_lshlrev_b32 v40, 16, v236\n"
      "v_and_b32 v41, s53, v236\n"
      "v_lshlrev_b32 v42, 16, v237\n"
      "v_and_b32 v43, s53, v237\n"
      "v_mfma_f32_16x16x32_bf16 v[28:31], a[160:163], v[224:227], 0\n"
      "v_mul_f32 v40, s40, v40\n"
      "v_mul_f32 v41, s40, v41\n"
      "v_mfma_f32_16x16x32_bf16 a[232:235], a[140:143], a[180:183], 0\n"
      "v_mul_f32 v42, s40, v42\n"
      "v_mul_f32 v43, s40, v43\n"
      "v_cvt_pk_bf16_f32 v236, v40, v41\n"
      "v_cvt_pk_bf16_f32 v237, v42, v43\n"
      "v_lshlrev_b32 v40, 16, v238\n"
      "v_and_b32 v41, s53, v238\n"
      "v_lshlrev_b32 v42, 16, v239\n"
      "v_and_b32 v43, s53, v239\n"
      "v_mfma_f32_16x16x32_bf16 v[28:31], a[164:167], v[228:231], v[28:31]\n"
      "v_mul_f32 v40, s40, v40\n"
      "v_mul_f32 v41, s40, v41\n"
      "v_mfma_f32_16x16x32_bf16 a[232:235], a[140:143], a[180:183], 0\n"
      "v_mul_f32 v42, s40, v42\n"
      "v_mul_f32 v43, s40, v43\n"
      "v_cvt_pk_bf16_f32 v238, v40, v41\n"
      "v_cvt_pk_bf16_f32 v239, v42, v43\n"
      "v_max3_f32 v124, v16, v17, v18\n"
      "v_max3_f32 v125, v19, v20, v21\n"
      "v_max3_f32 v124, v124, v22, v23\n"
      "v_max_f32 v124, v124, v125\n"
      "v_mov_b32 v125, v124\n"
      "s_nop 1\n"
      "v_permlane32_swap_b32 v124, v125\n"
      "v_max_f32 v124, v124, v125\n"
      "v_mov_b32 v125, v124\n"
      "s_nop 1\n"
      "v_permlane16_swap_b32 v124, v125\n"
      "v_max_f32 v124, v124, v125\n"
      "v_sub_f32 v16, v16, v124\n"
      "v_sub_f32 v17, v17, v124\n"
      "v_sub_f32 v18, v18, v124\n"
      "v_sub_f32 v19, v19, v124\n"
      "v_sub_f32 v20, v20, v124\n"
      "v_sub_f32 v21, v21, v124\n"
      "v_sub_f32 v22, v22, v124\n"
      "v_sub_f32 v23, v23, v124\n"
      "v_sub_f32 v104, 0, v124\n"
      "v_sub_f32 v105, 0, v124\n"
      "v_sub_f32 v106, 0, v124\n"
      "v_sub_f32 v107, 0, v124\n"
      "v_mov_b32 v114, 0\n"
      "s_add_u32 m0, s92, 65536\n"
      "s_nop 0\n"
      "buffer_load_dwordx4 v120, s[24:27], 0 offen offset:0 nt lds\n"
      "s_add_u32 m0, s92, 66432\n"
      "s_nop 0\n"
      "buffer_load_dwordx4 v120, s[24:27], 0 offen offset:128 nt lds\n"
      "s_add_u32 m0, s92, 67328\n"
      "s_nop 0\n"
      "buffer_load_dwordx4 v120, s[24:27], 0 offen offset:256 nt lds\n"
      "s_waitcnt vmcnt(11)\n"
      "ds_read_b128 v[248:251], v35 offset:3072\n"
      "ds_read_b128 v[252:255], v35 offset:3584\n"
      "s_waitcnt lgkmcnt(2)\n"
      "v_lshlrev_b32 v40, 16, v240\n"
      "v_and_b32 v41, s53, v240\n"
      "v_lshlrev_b32 v42, 16, v241\n"
      "v_and_b32 v43, s53, v241\n"
      "v_mfma_f32_16x16x32_bf16 v[24:27], a[136:139], v[232:235], v[24:27]\n"
      "v_mul_f32 v40, s40, v40\n"
      "v_mul_f32 v41, s40, v41\n"
      "v_mfma_f32_16x16x32_bf16 a[192:195], a[144:147], a[184:187], 0\n"
      "v_mul_f32 v42, s40, v42\n"
      "v_mul_f32 v43, s40, v43\n"
      "v_cvt_pk_bf16_f32 v240, v40, v41\n"
      "v_cvt_pk_bf16_f32 v241, v42, v43\n"
      "v_lshlrev_b32 v40, 16, v242\n"
      "v_and_b32 v41, s53, v242\n"
      "v_lshlrev_b32 v42, 16, v243\n"
      "v_and_b32 v43, s53, v243\n"
      "v_mfma_f32_16x16x32_bf16 v[24:27], a[140:143], v[236:239], v[24:27]\n"
      "v_mul_f32 v40, s40, v40\n"
      "v_mul_f32 v41, s40, v41\n"
      "v_mfma_f32_16x16x32_bf16 a[192:195], a[144:147], a[184:187], 0\n"
      "v_mul_f32 v42, s40, v42\n"
      "v_mul_f32 v43, s40, v43\n"
      "v_cvt_pk_bf16_f32 v242, v40, v41\n"
      "v_cvt_pk_bf16_f32 v243, v42, v43\n"
      "v_lshlrev_b32 v40, 16, v244\n"
      "v_and_b32 v41, s53, v244\n"
      "v_lshlrev_b32 v42, 16, v245\n"
      "v_and_b32 v43, s53, v245\n"
      "v_mfma_f32_16x16x32_bf16 v[28:31], a[168:171], v[232:235], v[28:31]\n"
      "v_mul_f32 v40, s40, v40\n"
      "v_mul_f32 v41, s40, v41\n"
      "v_mfma_f32_16x16x32_bf16 a[192:195], a[148:151], a[184:187], 0\n"
      "v_mul_f32 v42, s40, v42\n"
      "v_mul_f32 v43, s40, v43\n"
      "v_cvt_pk_bf16_f32 v244, v40, v41\n"
      "v_cvt_pk_bf16_f32 v245, v42, v43\n"
      "v_lshlrev_b32 v40, 16, v246\n"
      "v_and_b32 v41, s53, v246\n"
      "v_lshlrev_b32 v42, 16, v247\n"
      "v_and_b32 v43, s53, v247\n"
      "v_mfma_f32_16x16x32_bf16 v[28:31], a[172:175], v[236:239], v[28:31]\n"
      "v_mul_f32 v40, s40, v40\n"
      "v_mul_f32 v41, s40, v41\n"
      "v_mfma_f32_16x16x32_bf16 a[192:195], a[148:151], a[184:187], 0\n"
      "v_mul_f32 v42, s40, v42\n"
      "v_mul_f32 v43, s40, v43\n"
      "v_cvt_pk_bf16_f32 v246, v40, v41\n"
      "v_cvt_pk_bf16_f32 v247, v42, v43\n"
      "s_add_u32 m0, s92, 68224\n"
      "s_nop 0\n"
      "buffer_load_dwordx4 v120, s[24:27], 0 offen offset:384 nt lds\n"
      "s_mov_b32 s52, 32768\n"
      "s_add_u32 m0, s92, 32768\n"
      "s_nop 0\n"
      "buffer_load_dwordx4 v119, s[20:23], s52 offen offset:0 nt lds\n"
      "s_add_u32 m0, s92, 33664\n"
      "s_nop 0\n"
      "buffer_load_dwordx4 v119, s[20:23], s52 offen offset:128 nt lds\n"
      "s_waitcnt lgkmcnt(0)\n"
      "s_add_u32 m0, s92, 34560\n"
      "s_nop 0\n"
      "buffer_load_dwordx4 v119, s[20:23], s52 offen offset:256 nt lds\n"
      "s_add_u32 m0, s92, 35456\n"
      "s_nop 0\n"
      "buffer_load_dwordx4 v119, s[20:23], s52 offen offset:384 nt lds\n"
      "s_waitcnt vmcnt(12)\n"
      "s_barrier\n"
      "ds_read_b128 v[64:67], v116 offset:16384\n"
      "ds_read_b128 v[68:71], v116 offset:16896\n"
      "ds_read_b128 v[72:75], v116 offset:17408\n"
      "ds_read_b128 v[76:79], v116 offset:17920\n"
      "v_lshlrev_b32 v40, 16, v248\n"
      "v_and_b32 v41, s53, v248\n"
      "v_lshlrev_b32 v42, 16, v249\n"
      "v_and_b32 v43, s53, v249\n"
      "v_mfma_f32_16x16x32_bf16 v[24:27], a[144:147], v[240:243], v[24:27]\n"
      "v_mul_f32 v40, s40, v40\n"
      "v_mul_f32 v41, s40, v41\n"
      "v_mul_f32 v42, s40, v42\n"
      "v_mul_f32 v43, s40, v43\n"
      "v_cvt_pk_bf16_f32 v248, v40, v41\n"
      "v_cvt_pk_bf16_f32 v249, v42, v43\n"
      "v_lshlrev_b32 v40, 16, v250\n"
      "v_and_b32 v41, s53, v250\n"
      "v_lshlrev_b32 v42, 16, v251\n"
      "v_and_b32 v43, s53, v251\n"
      "v_mfma_f32_16x16x32_bf16 v[24:27], a[148:151], v[244:247], v[24:27]\n"
      "v_mul_f32 v40, s40, v40\n"
      "v_mul_f32 v41, s40, v41\n"
      "v_mul_f32 v42, s40, v42\n"
      "v_mul_f32 v43, s40, v43\n"
      "v_cvt_pk_bf16_f32 v250, v40, v41\n"
      "v_cvt_pk_bf16_f32 v251, v42, v43\n"
      "v_lshlrev_b32 v40, 16, v252\n"
      "v_and_b32 v41, s53, v252\n"
      "v_lshlrev_b32 v42, 16, v253\n"
      "v_and_b32 v43, s53, v253\n"
      "v_mfma_f32_16x16x32_bf16 v[28:31], a[176:179], v[240:243], v[28:31]\n"
      "v_mul_f32 v40, s40, v40\n"
      "v_mul_f32 v41, s40, v41\n"
      "v_mul_f32 v42, s40, v42\n"
      "v_mul_f32 v43, s40, v43\n"
      "v_cvt_pk_bf16_f32 v252, v40, v41\n"
      "v_cvt_pk_bf16_f32 v253, v42, v43\n"
      "v_lshlrev_b32 v40, 16, v254\n"
      "v_and_b32 v41, s53, v254\n"
      "v_lshlrev_b32 v42, 16, v255\n"
      "v_and_b32 v43, s53, v255\n"
      "v_mfma_f32_16x16x32_bf16 v[28:31], a[180:183], v[244:247], v[28:31]\n"
      "v_mul_f32 v40, s40, v40\n"
      "v_mul_f32 v41, s40, v41\n"
      "v_mul_f32 v42, s40, v42\n"
      "v_mul_f32 v43, s40, v43\n"
      "v_cvt_pk_bf16_f32 v254, v40, v41\n"
      "v_cvt_pk_bf16_f32 v255, v42, v43\n"
      "s_nop 0\n"
      "v_mfma_f32_16x16x32_bf16 v[24:27], a[152:155], v[248:251], v[24:27]\n"
      "v_mfma_f32_16x16x32_bf16 v[24:27], a[156:159], v[252:255], v[24:27]\n"
      "v_mfma_f32_16x16x32_bf16 v[28:31], a[184:187], v[248:251], v[28:31]\n"
      "v_mfma_f32_16x16x32_bf16 v[28:31], a[188:191], v[252:255], v[28:31]\n"
      "s_mov_b32 s36, 98304\n"
      "s_mov_b32 s37, 81920\n"
      "s_mov_b32 s39, 6\n"
      "s_nop 4\n"
      "v_max3_f32 v124, v24, v25, v26\n"
      "v_max3_f32 v125, v27, v28, v29\n"
      "v_max3_f32 v124, v124, v30, v31\n"
      "v_max_f32 v124, v124, v125\n"
      "v_mov_b32 v125, v124\n"
      "s_nop 1\n"
      "v_permlane32_swap_b32 v124, v125\n"
      "v_max_f32 v124, v124, v125\n"
      "v_mov_b32 v125, v124\n"
      "s_nop 1\n"
      "v_permlane16_swap_b32 v124, v125\n"
      "v_max_f32 v124, v124, v125\n"
      "v_add_f32 v124, s41, v124\n"
      "v_sub_f32 v24, v24, v124\n"
      "v_sub_f32 v25, v25, v124\n"
      "v_sub_f32 v26, v26, v124\n"
      "v_sub_f32 v27, v27, v124\n"
      "v_sub_f32 v28, v28, v124\n"
      "v_sub_f32 v29, v29, v124\n"
      "v_sub_f32 v30, v30, v124\n"
      "v_sub_f32 v31, v31, v124\n"
      "v_sub_f32 v108, 0, v124\n"
      "v_sub_f32 v109, 0, v124\n"
      "v_sub_f32 v110, 0, v124\n"
      "v_sub_f32 v111, 0, v124\n"
      "v_mov_b32 v115, 0\n"
      "s_waitcnt lgkmcnt(2)\n"
      "s_nop 0\n"
      "MAIN%=:\n"
      "v_mfma_f32_16x16x32_bf16 v[32:35], v[64:67], v[128:131], v[96:99]\n"
      "v_exp_f32 v16, v16\n"
      "v_mfma_f32_16x16x32_bf16 v[32:35], v[68:71], v[132:135], v[32:35]\n"
      "ds_read_b128 v[80:83], v116 offset:18432\n"
      "v_accvgpr_write_b32 a96, v224\n"
      "v_mfma_f32_16x16x32_bf16 v[40:43], v[68:71], v[164:167], v[100:103]\n"
      "v_add_f32 v114, v114, v16\n"
      "v_mfma_f32_16x16x32_bf16 v[40:43], v[64:67], v[160:163], v[40:43]\n"
      "ds_read_b128 v[84:87], v116 offset:18944\n"
      "v_accvgpr_write_b32 a97, v225\n"
      "v_mfma_f32_16x16x32_bf16 v[48:51], v[64:67], v[192:195], v[104:107]\n"
      "v_exp_f32 v17, v17\n"
      "v_mfma_f32_16x16x32_bf16 v[48:51], v[68:71], v[196:199], v[48:51]\n"
      "s_mov_b32 s52, 16384\n"
      "v_accvgpr_write_b32 a98, v226\n"
      "v_accvgpr_write_b32 a99, v227\n"
      "v_mfma_f32_16x16x32_bf16 v[56:59], v[68:71], v[228:231], v[108:111]\n"
      "s_add_u32 m0, s92, 81920\n"
      "v_add_f32 v114, v114, v17\n"
      "v_cvt_pk_bf16_f32 v16, v16, v17\n"
      "v_mfma_f32_16x16x32_bf16 v[56:59], v[64:67], v[224:227], v[56:59]\n"
      "s_waitcnt lgkmcnt(2)\n"
      "buffer_load_dwordx4 v120, s[24:27], s52 offen offset:0 nt lds\n"
      "v_mfma_f32_16x16x32_bf16 v[56:59], v[72:75], v[232:235], v[56:59]\n"
      "v_exp_f32 v18, v18\n"
      "v_mfma_f32_16x16x32_bf16 v[56:59], v[76:79], v[236:239], v[56:59]\n"
      "ds_read_b128 v[88:91], v116 offset:19456\n"
      "v_accvgpr_write_b32 a100, v228\n"
      "v_mfma_f32_16x16x32_bf16 v[48:51], v[76:79], v[204:207], v[48:51]\n"
      "v_add_f32 v114, v114, v18\n"
      "v_mfma_f32_16x16x32_bf16 v[48:51], v[72:75], v[200:203], v[48:51]\n"
      "ds_read_b128 v[92:95], v116 offset:19968\n"
      "v_accvgpr_write_b32 a101, v229\n"
      "v_mfma_f32_16x16x32_bf16 v[40:43], v[72:75], v[168:171], v[40:43]\n"
      "v_exp_f32 v19, v19\n"
      "v_mfma_f32_16x16x32_bf16 v[40:43], v[76:79], v[172:175], v[40:43]\n"
      "v_accvgpr_write_b32 a102, v230\n"
      "v_accvgpr_write_b32 a103, v231\n"
      "v_mfma_f32_16x16x32_bf16 v[32:35], v[76:79], v[140:143], v[32:35]\n"
      "s_add_u32 m0, s92, 82816\n"
      "v_add_f32 v114, v114, v19\n"
      "v_cvt_pk_bf16_f32 v17, v18, v19\n"
      "v_mfma_f32_16x16x32_bf16 v[32:35], v[72:75], v[136:139], v[32:35]\n"
      "s_waitcnt lgkmcnt(2)\n"
      "buffer_load_dwordx4 v120, s[24:27], s52 offen offset:128 nt lds\n"
      "v_mfma_f32_16x16x32_bf16 v[32:35], v[80:83], v[144:147], v[32:35]\n"
      "v_exp_f32 v20, v20\n"
      "v_mfma_f32_16x16x32_bf16 v[32:35], v[84:87], v[148:151], v[32:35]\n"
      "ds_read_b128 v[64:67], v116 offset:24576\n"
      "v_accvgpr_write_b32 a104, v232\n"
      "v_mfma_f32_16x16x32_bf16 v[40:43], v[84:87], v[180:183], v[40:43]\n"
      "v_add_f32 v114, v114, v20\n"
      "v_mfma_f32_16x16x32_bf16 v[40:43], v[80:83], v[176:179], v[40:43]\n"
      "ds_read_b128 v[68:71], v116 offset:25088\n"
      "v_accvgpr_write_b32 a105, v233\n"
      "v_mfma_f32_16x16x32_bf16 v[48:51], v[80:83], v[208:211], v[48:51]\n"
      "v_exp_f32 v21, v21\n"
      "v_mfma_f32_16x16x32_bf16 v[48:51], v[84:87], v[212:215], v[48:51]\n"
      "v_accvgpr_write_b32 a106, v234\n"
      "v_accvgpr_write_b32 a107, v235\n"
      "v_mfma_f32_16x16x32_bf16 v[56:59], v[84:87], v[244:247], v[56:59]\n"
      "s_add_u32 m0, s92, 83712\n"
      "v_add_f32 v114, v114, v21\n"
      "v_cvt_pk_bf16_f32 v18, v20, v21\n"
      "v_mfma_f32_16x16x32_bf16 v[56:59], v[80:83], v[240:243], v[56:59]\n"
      "s_waitcnt lgkmcnt(2)\n"
      "buffer_load_dwordx4 v120, s[24:27], s52 offen offset:256 nt lds\n"
      "v_mfma_f32_16x16x32_bf16 v[56:59], v[88:91], v[248:251], v[56:59]\n"
      "v_exp_f32 v22, v22\n"
      "v_mfma_f32_16x16x32_bf16 v[56:59], v[92:95], v[252:255], v[56:59]\n"
      "ds_read_b128 v[72:75], v116 offset:25600\n"
      "v_accvgpr_write_b32 a108, v236\n"
      "v_mfma_f32_16x16x32_bf16 v[48:51], v[92:95], v[220:223], v[48:51]\n"
      "v_add_f32 v114, v114, v22\n"
      "v_mfma_f32_16x16x32_bf16 v[48:51], v[88:91], v[216:219], v[48:51]\n"
      "ds_read_b128 v[76:79], v116 offset:26112\n"
      "v_accvgpr_write_b32 a109, v237\n"
      "v_mfma_f32_16x16x32_bf16 v[40:43], v[88:91], v[184:187], v[40:43]\n"
      "v_exp_f32 v23, v23\n"
      "v_mfma_f32_16x16x32_bf16 v[40:43], v[92:95], v[188:191], v[40:43]\n"
      "v_accvgpr_write_b32 a110, v238\n"
      "v_accvgpr_write_b32 a111, v239\n"
      "v_mfma_f32_16x16x32_bf16 v[32:35], v[92:95], v[156:159], v[32:35]\n"
      "s_add_u32 m0, s92, 84608\n"
      "v_add_f32 v114, v114, v23\n"
      "v_cvt_pk_bf16_f32 v19, v22, v23\n"
      "v_mfma_f32_16x16x32_bf16 v[32:35], v[88:91], v[152:155], v[32:35]\n"
      "s_waitcnt lgkmcnt(2)\n"
      "buffer_load_dwordx4 v120, s[24:27], s52 offen offset:384 nt lds\n"
      "v_mfma_f32_16x16x32_bf16 v[36:39], v[64:67], v[128:131], v[96:99]\n"
      "v_exp_f32 v24, v24\n"
      "v_mfma_f32_16x16x32_bf16 v[36:39], v[68:71], v[132:135], v[36:39]\n"
      "ds_read_b128 v[80:83], v116 offset:26624\n"
      "v_accvgpr_write_b32 a112, v240\n"
      "v_mfma_f32_16x16x32_bf16 v[44:47], v[68:71], v[164:167], v[100:103]\n"
      "v_add_f32 v115, v115, v24\n"
      "v_mfma_f32_16x16x32_bf16 v[44:47], v[64:67], v[160:163], v[44:47]\n"
      "ds_read_b128 v[84:87], v116 offset:27136\n"
      "v_accvgpr_write_b32 a113, v241\n"
      "v_mfma_f32_16x16x32_bf16 v[52:55], v[64:67], v[192:195], v[104:107]\n"
      "v_exp_f32 v25, v25\n"
      "v_mfma_f32_16x16x32_bf16 v[52:55], v[68:71], v[196:199], v[52:55]\n"
      "s_mov_b32 s52, 49152\n"
      "v_accvgpr_write_b32 a114, v242\n"
      "v_accvgpr_write_b32 a115, v243\n"
      "v_mfma_f32_16x16x32_bf16 v[60:63], v[68:71], v[228:231], v[108:111]\n"
      "s_add_u32 m0, s92, 49152\n"
      "v_add_f32 v115, v115, v25\n"
      "v_cvt_pk_bf16_f32 v24, v24, v25\n"
      "v_mfma_f32_16x16x32_bf16 v[60:63], v[64:67], v[224:227], v[60:63]\n"
      "s_waitcnt lgkmcnt(2)\n"
      "buffer_load_dwordx4 v119, s[20:23], s52 offen offset:0 nt lds\n"
      "v_mfma_f32_16x16x32_bf16 v[60:63], v[72:75], v[232:235], v[60:63]\n"
      "v_exp_f32 v26, v26\n"
      "v_mfma_f32_16x16x32_bf16 v[60:63], v[76:79], v[236:239], v[60:63]\n"
      "ds_read_b128 v[88:91], v116 offset:27648\n"
      "v_accvgpr_write_b32 a116, v244\n"
      "v_mfma_f32_16x16x32_bf16 v[52:55], v[76:79], v[204:207], v[52:55]\n"
      "v_add_f32 v115, v115, v26\n"
      "v_mfma_f32_16x16x32_bf16 v[52:55], v[72:75], v[200:203], v[52:55]\n"
      "ds_read_b128 v[92:95], v116 offset:28160\n"
      "v_accvgpr_write_b32 a117, v245\n"
      "v_mfma_f32_16x16x32_bf16 v[44:47], v[72:75], v[168:171], v[44:47]\n"
      "s_waitcnt vmcnt(9)\n"
      "s_barrier\n"
      "v_exp_f32 v27, v27\n"
      "v_mfma_f32_16x16x32_bf16 v[44:47], v[76:79], v[172:175], v[44:47]\n"
      "v_accvgpr_write_b32 a118, v246\n"
      "v_accvgpr_write_b32 a119, v247\n"
      "v_mfma_f32_16x16x32_bf16 v[36:39], v[76:79], v[140:143], v[36:39]\n"
      "s_add_u32 m0, s92, 50048\n"
      "v_add_f32 v115, v115, v27\n"
      "v_cvt_pk_bf16_f32 v25, v26, v27\n"
      "v_mfma_f32_16x16x32_bf16 v[36:39], v[72:75], v[136:139], v[36:39]\n"
      "s_waitcnt lgkmcnt(2)\n"
      "buffer_load_dwordx4 v119, s[20:23], s52 offen offset:128 nt lds\n"
      "v_mfma_f32_16x16x32_bf16 v[36:39], v[80:83], v[144:147], v[36:39]\n"
      "v_exp_f32 v28, v28\n"
      "v_mfma_f32_16x16x32_bf16 v[36:39], v[84:87], v[148:151], v[36:39]\n"
      "ds_read_b64_tr_b16 v[64:65], v117 offset:0\n"
      "v_accvgpr_write_b32 a120, v248\n"
      "v_mfma_f32_16x16x32_bf16 v[44:47], v[84:87], v[180:183], v[44:47]\n"
      "ds_read_b64_tr_b16 v[66:67], v117 offset:8192\n"
      "v_add_f32 v115, v115, v28\n"
      "v_mfma_f32_16x16x32_bf16 v[44:47], v[80:83], v[176:179], v[44:47]\n"
      "ds_read_b64_tr_b16 v[68:69], v118 offset:0\n"
      "v_accvgpr_write_b32 a121, v249\n"
      "v_mfma_f32_16x16x32_bf16 v[52:55], v[80:83], v[208:211], v[52:55]\n"
      "v_exp_f32 v29, v29\n"
      "v_mfma_f32_16x16x32_bf16 v[52:55], v[84:87], v[212:215], v[52:55]\n"
      "ds_read_b64_tr_b16 v[70:71], v118 offset:8192\n"
      "v_accvgpr_write_b32 a122, v250\n"
      "v_mfma_f32_16x16x32_bf16 v[60:63], v[84:87], v[244:247], v[60:63]\n"
      "s_add_u32 m0, s92, 50944\n"
      "v_add_f32 v115, v115, v29\n"
      "v_cvt_pk_bf16_f32 v26, v28, v29\n"
      "v_accvgpr_write_b32 a123, v251\n"
      "v_mfma_f32_16x16x32_bf16 v[60:63], v[80:83], v[240:243], v[60:63]\n"
      "s_waitcnt lgkmcnt(4)\n"
      "buffer_load_dwordx4 v119, s[20:23], s52 offen offset:256 nt lds\n"
      "v_mfma_f32_16x16x32_bf16 v[60:63], v[88:91], v[248:251], v[60:63]\n"
      "v_exp_f32 v30, v30\n"
      "v_exp_f32 v31, v31\n"
      "v_exp_f32 v32, v32\n"
      "v_mfma_f32_16x16x32_bf16 v[60:63], v[92:95], v[252:255], v[60:63]\n"
      "ds_read_b64_tr_b16 v[72:73], v117 offset:512\n"
      "v_accvgpr_write_b32 a124, v252\n"
      "v_mfma_f32_16x16x32_bf16 v[52:55], v[92:95], v[220:223], v[52:55]\n"
      "ds_read_b64_tr_b16 v[74:75], v117 offset:8704\n"
      "v_add_f32 v115, v115, v30\n"
      "v_add_f32 v115, v115, v31\n"
      "v_cvt_pk_bf16_f32 v27, v30, v31\n"
      "v_add_f32 v112, v112, v32\n"
      "v_mfma_f32_16x16x32_bf16 v[52:55], v[88:91], v[216:219], v[52:55]\n"
      "ds_read_b64_tr_b16 v[76:77], v118 offset:512\n"
      "v_accvgpr_write_b32 a125, v253\n"
      "v_mfma_f32_16x16x32_bf16 v[44:47], v[88:91], v[184:187], v[44:47]\n"
      "v_exp_f32 v33, v33\n"
      "v_mfma_f32_16x16x32_bf16 v[44:47], v[92:95], v[188:191], v[44:47]\n"
      "ds_read_b64_tr_b16 v[78:79], v118 offset:8704\n"
      "v_accvgpr_write_b32 a126, v254\n"
      "v_mfma_f32_16x16x32_bf16 v[36:39], v[92:95], v[156:159], v[36:39]\n"
      "s_add_u32 m0, s92, 51840\n"
      "v_add_f32 v112, v112, v33\n"
      "v_cvt_pk_bf16_f32 v32, v32, v33\n"
      "v_accvgpr_write_b32 a127, v255\n"
      "v_mfma_f32_16x16x32_bf16 v[36:39], v[88:91], v[152:155], v[36:39]\n"
      "s_waitcnt lgkmcnt(4)\n"
      "buffer_load_dwordx4 v119, s[20:23], s52 offen offset:384 nt lds\n"
      "v_mfma_f32_16x16x32_bf16 v[128:131], v[0:3], v[64:67], 0\n"
      "v_exp_f32 v34, v34\n"
      "v_mfma_f32_16x16x32_bf16 v[132:135], v[0:3], v[68:71], 0\n"
      "ds_read_b64_tr_b16 v[80:81], v117 offset:1024\n"
      "v_mfma_f32_16x16x32_bf16 v[196:199], v[8:11], v[68:71], 0\n"
      "ds_read_b64_tr_b16 v[82:83], v117 offset:9216\n"
      "v_add_f32 v112, v112, v34\n"
      "v_mfma_f32_16x16x32_bf16 v[192:195], v[8:11], v[64:67], 0\n"
      "ds_read_b64_tr_b16 v[84:85], v118 offset:1024\n"
      "v_mfma_f32_16x16x32_bf16 a[128:131], v[16:19], v[64:67], 0\n"
      "v_exp_f32 v35, v35\n"
      "v_mfma_f32_16x16x32_bf16 a[132:135], v[16:19], v[68:71], 0\n"
      "s_mov_b32 s52, 32768\n"
      "ds_read_b64_tr_b16 v[86:87], v118 offset:9216\n"
      "v_mfma_f32_16x16x32_bf16 a[196:199], v[24:27], v[68:71], 0\n"
      "s_add_u32 m0, s92, 98304\n"
      "v_add_f32 v112, v112, v35\n"
      "v_cvt_pk_bf16_f32 v33, v34, v35\n"
      "v_mfma_f32_16x16x32_bf16 a[192:195], v[24:27], v[64:67], 0\n"
      "s_waitcnt lgkmcnt(4)\n"
      "buffer_load_dwordx4 v120, s[24:27], s52 offen offset:0 nt lds\n"
      "v_mfma_f32_16x16x32_bf16 a[200:203], v[24:27], v[72:75], 0\n"
      "v_exp_f32 v36, v36\n"
      "v_mfma_f32_16x16x32_bf16 a[204:207], v[24:27], v[76:79], 0\n"
      "ds_read_b64_tr_b16 v[88:89], v117 offset:1536\n"
      "v_mfma_f32_16x16x32_bf16 a[140:143], v[16:19], v[76:79], 0\n"
      "ds_read_b64_tr_b16 v[90:91], v117 offset:9728\n"
      "v_add_f32 v112, v112, v36\n"
      "v_mfma_f32_16x16x32_bf16 a[136:139], v[16:19], v[72:75], 0\n"
      "ds_read_b64_tr_b16 v[92:93], v118 offset:1536\n"
      "v_mfma_f32_16x16x32_bf16 v[200:203], v[8:11], v[72:75], 0\n"
      "v_exp_f32 v37, v37\n"
      "v_mfma_f32_16x16x32_bf16 v[204:207], v[8:11], v[76:79], 0\n"
      "ds_read_b64_tr_b16 v[94:95], v118 offset:9728\n"
      "v_mfma_f32_16x16x32_bf16 v[140:143], v[0:3], v[76:79], 0\n"
      "s_add_u32 m0, s92, 99200\n"
      "v_add_f32 v112, v112, v37\n"
      "v_cvt_pk_bf16_f32 v34, v36, v37\n"
      "v_mfma_f32_16x16x32_bf16 v[136:139], v[0:3], v[72:75], 0\n"
      "s_waitcnt lgkmcnt(4)\n"
      "buffer_load_dwordx4 v120, s[24:27], s52 offen offset:128 nt lds\n"
      "v_mfma_f32_16x16x32_bf16 v[144:147], v[0:3], v[80:83], 0\n"
      "v_exp_f32 v38, v38\n"
      "v_mfma_f32_16x16x32_bf16 v[148:151], v[0:3], v[84:87], 0\n"
      "ds_read_b64_tr_b16 v[64:65], v117 offset:2048\n"
      "v_mfma_f32_16x16x32_bf16 v[212:215], v[8:11], v[84:87], 0\n"
      "ds_read_b64_tr_b16 v[66:67], v117 offset:10240\n"
      "v_add_f32 v112, v112, v38\n"
      "v_mfma_f32_16x16x32_bf16 v[208:211], v[8:11], v[80:83], 0\n"
      "ds_read_b64_tr_b16 v[68:69], v118 offset:2048\n"
      "v_mfma_f32_16x16x32_bf16 a[144:147], v[16:19], v[80:83], 0\n"
      "v_exp_f32 v39, v39\n"
      "v_mfma_f32_16x16x32_bf16 a[148:151], v[16:19], v[84:87], 0\n"
      "ds_read_b64_tr_b16 v[70:71], v118 offset:10240\n"
      "v_mfma_f32_16x16x32_bf16 a[212:215], v[24:27], v[84:87], 0\n"
      "s_add_u32 m0, s92, 100096\n"
      "v_add_f32 v112, v112, v39\n"
      "v_cvt_pk_bf16_f32 v35, v38, v39\n"
      "v_mfma_f32_16x16x32_bf16 a[208:211], v[24:27], v[80:83], 0\n"
      "s_waitcnt lgkmcnt(4)\n"
      "buffer_load_dwordx4 v120, s[24:27], s52 offen offset:256 nt lds\n"
      "v_mfma_f32_16x16x32_bf16 a[216:219], v[24:27], v[88:91], 0\n"
      "v_exp_f32 v40, v40\n"
      "v_mfma_f32_16x16x32_bf16 a[220:223], v[24:27], v[92:95], 0\n"
      "ds_read_b64_tr_b16 v[72:73], v117 offset:2560\n"
      "v_mfma_f32_16x16x32_bf16 a[156:159], v[16:19], v[92:95], 0\n"
      "ds_read_b64_tr_b16 v[74:75], v117 offset:10752\n"
      "v_add_f32 v113, v113, v40\n"
      "v_mfma_f32_16x16x32_bf16 a[152:155], v[16:19], v[88:91], 0\n"
      "ds_read_b64_tr_b16 v[76:77], v118 offset:2560\n"
      "v_mfma_f32_16x16x32_bf16 v[216:219], v[8:11], v[88:91], 0\n"
      "v_exp_f32 v41, v41\n"
      "v_mfma_f32_16x16x32_bf16 v[220:223], v[8:11], v[92:95], 0\n"
      "ds_read_b64_tr_b16 v[78:79], v118 offset:10752\n"
      "v_mfma_f32_16x16x32_bf16 v[156:159], v[0:3], v[92:95], 0\n"
      "s_add_u32 m0, s92, 100992\n"
      "v_add_f32 v113, v113, v41\n"
      "v_cvt_pk_bf16_f32 v40, v40, v41\n"
      "v_mfma_f32_16x16x32_bf16 v[152:155], v[0:3], v[88:91], 0\n"
      "s_waitcnt lgkmcnt(4)\n"
      "buffer_load_dwordx4 v120, s[24:27], s52 offen offset:384 nt lds\n"
      "v_mfma_f32_16x16x32_bf16 v[160:163], v[0:3], v[64:67], 0\n"
      "v_exp_f32 v42, v42\n"
      "v_mfma_f32_16x16x32_bf16 v[164:167], v[0:3], v[68:71], 0\n"
      "ds_read_b64_tr_b16 v[80:81], v117 offset:3072\n"
      "v_mfma_f32_16x16x32_bf16 v[228:231], v[8:11], v[68:71], 0\n"
      "ds_read_b64_tr_b16 v[82:83], v117 offset:11264\n"
      "v_add_f32 v113, v113, v42\n"
      "v_mfma_f32_16x16x32_bf16 v[224:227], v[8:11], v[64:67], 0\n"
      "ds_read_b64_tr_b16 v[84:85], v118 offset:3072\n"
      "v_mfma_f32_16x16x32_bf16 a[160:163], v[16:19], v[64:67], 0\n"
      "v_exp_f32 v43, v43\n"
      "v_mfma_f32_16x16x32_bf16 a[164:167], v[16:19], v[68:71], 0\n"
      "s_mov_b32 s52, 65536\n"
      "ds_read_b64_tr_b16 v[86:87], v118 offset:11264\n"
      "v_mfma_f32_16x16x32_bf16 a[228:231], v[24:27], v[68:71], 0\n"
      "s_add_u32 m0, s92, 0\n"
      "v_add_f32 v113, v113, v43\n"
      "v_cvt_pk_bf16_f32 v41, v42, v43\n"
      "v_mfma_f32_16x16x32_bf16 a[224:227], v[24:27], v[64:67], 0\n"
      "s_waitcnt lgkmcnt(4)\n"
      "buffer_load_dwordx4 v119, s[20:23], s52 offen offset:0 nt lds\n"
      "v_mfma_f32_16x16x32_bf16 a[232:235], v[24:27], v[72:75], 0\n"
      "v_exp_f32 v44, v44\n"
      "v_mfma_f32_16x16x32_bf16 a[236:239], v[24:27], v[76:79], 0\n"
      "ds_read_b64_tr_b16 v[88:89], v117 offset:3584\n"
      "v_mfma_f32_16x16x32_bf16 a[172:175], v[16:19], v[76:79], 0\n"
      "ds_read_b64_tr_b16 v[90:91], v117 offset:11776\n"
      "v_add_f32 v113, v113, v44\n"
      "v_mfma_f32_16x16x32_bf16 a[168:171], v[16:19], v[72:75], 0\n"
      "ds_read_b64_tr_b16 v[92:93], v118 offset:3584\n"
      "v_mfma_f32_16x16x32_bf16 v[232:235], v[8:11], v[72:75], 0\n"
      "s_waitcnt vmcnt(13)\n"
      "s_barrier\n"
      "v_exp_f32 v45, v45\n"
      "v_mfma_f32_16x16x32_bf16 v[236:239], v[8:11], v[76:79], 0\n"
      "ds_read_b64_tr_b16 v[94:95], v118 offset:11776\n"
      "v_mfma_f32_16x16x32_bf16 v[172:175], v[0:3], v[76:79], 0\n"
      "s_add_u32 m0, s92, 896\n"
      "v_add_f32 v113, v113, v45\n"
      "v_cvt_pk_bf16_f32 v42, v44, v45\n"
      "v_mfma_f32_16x16x32_bf16 v[168:171], v[0:3], v[72:75], 0\n"
      "s_waitcnt lgkmcnt(4)\n"
      "buffer_load_dwordx4 v119, s[20:23], s52 offen offset:128 nt lds\n"
      "v_mfma_f32_16x16x32_bf16 v[176:179], v[0:3], v[80:83], 0\n"
      "v_exp_f32 v46, v46\n"
      "v_mfma_f32_16x16x32_bf16 v[180:183], v[0:3], v[84:87], 0\n"
      "ds_read_b128 v[64:67], v116 offset:32768\n"
      "v_mfma_f32_16x16x32_bf16 v[244:247], v[8:11], v[84:87], 0\n"
      "v_add_f32 v113, v113, v46\n"
      "v_mfma_f32_16x16x32_bf16 v[240:243], v[8:11], v[80:83], 0\n"
      "ds_read_b128 v[68:71], v116 offset:33280\n"
      "v_mfma_f32_16x16x32_bf16 a[176:179], v[16:19], v[80:83], 0\n"
      "v_exp_f32 v47, v47\n"
      "v_mfma_f32_16x16x32_bf16 a[180:183], v[16:19], v[84:87], 0\n"
      "v_mfma_f32_16x16x32_bf16 a[244:247], v[24:27], v[84:87], 0\n"
      "s_add_u32 m0, s92, 1792\n"
      "v_add_f32 v113, v113, v47\n"
      "v_cvt_pk_bf16_f32 v43, v46, v47\n"
      "v_mfma_f32_16x16x32_bf16 a[240:243], v[24:27], v[80:83], 0\n"
      "s_waitcnt lgkmcnt(2)\n"
      "buffer_load_dwordx4 v119, s[20:23], s52 offen offset:256 nt lds\n"
      "v_mfma_f32_16x16x32_bf16 a[248:251], v[24:27], v[88:91], 0\n"
      "v_exp_f32 v48, v48\n"
      "v_mfma_f32_16x16x32_bf16 a[252:255], v[24:27], v[92:95], 0\n"
      "ds_read_b128 v[72:75], v116 offset:33792\n"
      "v_mfma_f32_16x16x32_bf16 a[188:191], v[16:19], v[92:95], 0\n"
      "v_add_f32 v114, v114, v48\n"
      "v_mfma_f32_16x16x32_bf16 a[184:187], v[16:19], v[88:91], 0\n"
      "ds_read_b128 v[76:79], v116 offset:34304\n"
      "v_mfma_f32_16x16x32_bf16 v[248:251], v[8:11], v[88:91], 0\n"
      "v_exp_f32 v49, v49\n"
      "v_mfma_f32_16x16x32_bf16 v[252:255], v[8:11], v[92:95], 0\n"
      "v_mfma_f32_16x16x32_bf16 v[188:191], v[0:3], v[92:95], 0\n"
      "s_add_u32 m0, s92, 2688\n"
      "v_add_f32 v114, v114, v49\n"
      "v_cvt_pk_bf16_f32 v48, v48, v49\n"
      "v_mfma_f32_16x16x32_bf16 v[184:187], v[0:3], v[88:91], 0\n"
      "s_waitcnt lgkmcnt(2)\n"
      "buffer_load_dwordx4 v119, s[20:23], s52 offen offset:384 nt lds\n"
      "v_mfma_f32_16x16x32_bf16 v[0:3], v[64:67], a[0:3], v[96:99]\n"
      "v_exp_f32 v50, v50\n"
      "v_mfma_f32_16x16x32_bf16 v[0:3], v[68:71], a[4:7], v[0:3]\n"
      "ds_read_b128 v[80:83], v116 offset:34816\n"
      "v_mfma_f32_16x16x32_bf16 v[8:11], v[68:71], a[36:39], v[100:103]\n"
      "v_add_f32 v114, v114, v50\n"
      "v_mfma_f32_16x16x32_bf16 v[8:11], v[64:67], a[32:35], v[8:11]\n"
      "ds_read_b128 v[84:87], v116 offset:35328\n"
      "v_mfma_f32_16x16x32_bf16 v[16:19], v[64:67], a[64:67], v[104:107]\n"
      "v_exp_f32 v51, v51\n"
      "v_mfma_f32_16x16x32_bf16 v[16:19], v[68:71], a[68:71], v[16:19]\n"
      "s_mov_b32 s52, 49152\n"
      "v_mfma_f32_16x16x32_bf16 v[24:27], v[68:71], a[100:103], v[108:111]\n"
      "s_add_u32 m0, s92, 114688\n"
      "v_add_f32 v114, v114, v51\n"
      "v_cvt_pk_bf16_f32 v49, v50, v51\n"
      "v_mfma_f32_16x16x32_bf16 v[24:27], v[64:67], a[96:99], v[24:27]\n"
      "s_waitcnt lgkmcnt(2)\n"
      "buffer_load_dwordx4 v120, s[24:27], s52 offen offset:0 nt lds\n"
      "v_mfma_f32_16x16x32_bf16 v[24:27], v[72:75], a[104:107], v[24:27]\n"
      "v_exp_f32 v52, v52\n"
      "v_mfma_f32_16x16x32_bf16 v[24:27], v[76:79], a[108:111], v[24:27]\n"
      "ds_read_b128 v[88:91], v116 offset:35840\n"
      "v_mfma_f32_16x16x32_bf16 v[16:19], v[76:79], a[76:79], v[16:19]\n"
      "v_add_f32 v114, v114, v52\n"
      "v_mfma_f32_16x16x32_bf16 v[16:19], v[72:75], a[72:75], v[16:19]\n"
      "ds_read_b128 v[92:95], v116 offset:36352\n"
      "v_mfma_f32_16x16x32_bf16 v[8:11], v[72:75], a[40:43], v[8:11]\n"
      "v_exp_f32 v53, v53\n"
      "v_mfma_f32_16x16x32_bf16 v[8:11], v[76:79], a[44:47], v[8:11]\n"
      "v_mfma_f32_16x16x32_bf16 v[0:3], v[76:79], a[12:15], v[0:3]\n"
      "s_add_u32 m0, s92, 115584\n"
      "v_add_f32 v114, v114, v53\n"
      "v_cvt_pk_bf16_f32 v50, v52, v53\n"
      "v_mfma_f32_16x16x32_bf16 v[0:3], v[72:75], a[8:11], v[0:3]\n"
      "s_waitcnt lgkmcnt(2)\n"
      "buffer_load_dwordx4 v120, s[24:27], s52 offen offset:128 nt lds\n"
      "v_mfma_f32_16x16x32_bf16 v[0:3], v[80:83], a[16:19], v[0:3]\n"
      "v_exp_f32 v54, v54\n"
      "v_mfma_f32_16x16x32_bf16 v[0:3], v[84:87], a[20:23], v[0:3]\n"
      "ds_read_b128 v[64:67], v116 offset:40960\n"
      "v_mfma_f32_16x16x32_bf16 v[8:11], v[84:87], a[52:55], v[8:11]\n"
      "v_add_f32 v114, v114, v54\n"
      "v_mfma_f32_16x16x32_bf16 v[8:11], v[80:83], a[48:51], v[8:11]\n"
      "ds_read_b128 v[68:71], v116 offset:41472\n"
      "v_mfma_f32_16x16x32_bf16 v[16:19], v[80:83], a[80:83], v[16:19]\n"
      "v_exp_f32 v55, v55\n"
      "v_mfma_f32_16x16x32_bf16 v[16:19], v[84:87], a[84:87], v[16:19]\n"
      "v_mfma_f32_16x16x32_bf16 v[24:27], v[84:87], a[116:119], v[24:27]\n"
      "s_add_u32 m0, s92, 116480\n"
      "v_add_f32 v114, v114, v55\n"
      "v_cvt_pk_bf16_f32 v51, v54, v55\n"
      "v_mfma_f32_16x16x32_bf16 v[24:27], v[80:83], a[112:115], v[24:27]\n"
      "s_waitcnt lgkmcnt(2)\n"
      "buffer_load_dwordx4 v120, s[24:27], s52 offen offset:256 nt lds\n"
      "v_mfma_f32_16x16x32_bf16 v[24:27], v[88:91], a[120:123], v[24:27]\n"
      "v_exp_f32 v56, v56\n"
      "v_mfma_f32_16x16x32_bf16 v[24:27], v[92:95], a[124:127], v[24:27]\n"
      "ds_read_b128 v[72:75], v116 offset:41984\n"
      "v_mfma_f32_16x16x32_bf16 v[16:19], v[92:95], a[92:95], v[16:19]\n"
      "v_add_f32 v115, v115, v56\n"
      "v_mfma_f32_16x16x32_bf16 v[16:19], v[88:91], a[88:91], v[16:19]\n"
      "ds_read_b128 v[76:79], v116 offset:42496\n"
      "v_mfma_f32_16x16x32_bf16 v[8:11], v[88:91], a[56:59], v[8:11]\n"
      "v_exp_f32 v57, v57\n"
      "v_mfma_f32_16x16x32_bf16 v[8:11], v[92:95], a[60:63], v[8:11]\n"
      "v_mfma_f32_16x16x32_bf16 v[0:3], v[92:95], a[28:31], v[0:3]\n"
      "s_add_u32 m0, s92, 117376\n"
      "v_add_f32 v115, v115, v57\n"
      "v_cvt_pk_bf16_f32 v56, v56, v57\n"
      "v_mfma_f32_16x16x32_bf16 v[0:3], v[88:91], a[24:27], v[0:3]\n"
      "s_waitcnt lgkmcnt(2)\n"
      "buffer_load_dwordx4 v120, s[24:27], s52 offen offset:384 nt lds\n"
      "v_mfma_f32_16x16x32_bf16 v[4:7], v[64:67], a[0:3], v[96:99]\n"
      "v_exp_f32 v58, v58\n"
      "v_mfma_f32_16x16x32_bf16 v[4:7], v[68:71], a[4:7], v[4:7]\n"
      "ds_read_b128 v[80:83], v116 offset:43008\n"
      "v_mfma_f32_16x16x32_bf16 v[12:15], v[68:71], a[36:39], v[100:103]\n"
      "v_add_f32 v115, v115, v58\n"
      "v_mfma_f32_16x16x32_bf16 v[12:15], v[64:67], a[32:35], v[12:15]\n"
      "ds_read_b128 v[84:87], v116 offset:43520\n"
      "v_mfma_f32_16x16x32_bf16 v[20:23], v[64:67], a[64:67], v[104:107]\n"
      "v_exp_f32 v59, v59\n"
      "v_mfma_f32_16x16x32_bf16 v[20:23], v[68:71], a[68:71], v[20:23]\n"
      "s_mov_b32 s52, 81920\n"
      "v_mfma_f32_16x16x32_bf16 v[28:31], v[68:71], a[100:103], v[108:111]\n"
      "s_add_u32 m0, s92, 16384\n"
      "v_add_f32 v115, v115, v59\n"
      "v_cvt_pk_bf16_f32 v57, v58, v59\n"
      "v_mfma_f32_16x16x32_bf16 v[28:31], v[64:67], a[96:99], v[28:31]\n"
      "s_waitcnt lgkmcnt(2)\n"
      "buffer_load_dwordx4 v119, s[20:23], s52 offen offset:0 nt lds\n"
      "v_mfma_f32_16x16x32_bf16 v[28:31], v[72:75], a[104:107], v[28:31]\n"
      "v_exp_f32 v60, v60\n"
      "v_mfma_f32_16x16x32_bf16 v[28:31], v[76:79], a[108:111], v[28:31]\n"
      "ds_read_b128 v[88:91], v116 offset:44032\n"
      "v_mfma_f32_16x16x32_bf16 v[20:23], v[76:79], a[76:79], v[20:23]\n"
      "v_add_f32 v115, v115, v60\n"
      "v_mfma_f32_16x16x32_bf16 v[20:23], v[72:75], a[72:75], v[20:23]\n"
      "ds_read_b128 v[92:95], v116 offset:44544\n"
      "v_mfma_f32_16x16x32_bf16 v[12:15], v[72:75], a[40:43], v[12:15]\n"
      "s_waitcnt vmcnt(17)\n"
      "s_barrier\n"
      "v_exp_f32 v61, v61\n"
      "v_mfma_f32_16x16x32_bf16 v[12:15], v[76:79], a[44:47], v[12:15]\n"
      "v_mfma_f32_16x16x32_bf16 v[4:7], v[76:79], a[12:15], v[4:7]\n"
      "s_add_u32 m0, s92, 17280\n"
      "v_add_f32 v115, v115, v61\n"
      "v_cvt_pk_bf16_f32 v58, v60, v61\n"
      "v_mfma_f32_16x16x32_bf16 v[4:7], v[72:75], a[8:11], v[4:7]\n"
      "s_waitcnt lgkmcnt(2)\n"
      "buffer_load_dwordx4 v119, s[20:23], s52 offen offset:128 nt lds\n"
      "v_mfma_f32_16x16x32_bf16 v[4:7], v[80:83], a[16:19], v[4:7]\n"
      "v_exp_f32 v62, v62\n"
      "v_mfma_f32_16x16x32_bf16 v[4:7], v[84:87], a[20:23], v[4:7]\n"
      "ds_read_b64_tr_b16 v[64:65], v117 offset:16384\n"
      "v_mfma_f32_16x16x32_bf16 v[12:15], v[84:87], a[52:55], v[12:15]\n"
      "ds_read_b64_tr_b16 v[66:67], v117 offset:24576\n"
      "v_add_f32 v115, v115, v62\n"
      "v_mfma_f32_16x16x32_bf16 v[12:15], v[80:83], a[48:51], v[12:15]\n"
      "ds_read_b64_tr_b16 v[68:69], v118 offset:16384\n"
      "v_mfma_f32_16x16x32_bf16 v[20:23], v[80:83], a[80:83], v[20:23]\n"
      "v_exp_f32 v63, v63\n"
      "v_mfma_f32_16x16x32_bf16 v[20:23], v[84:87], a[84:87], v[20:23]\n"
      "ds_read_b64_tr_b16 v[70:71], v118 offset:24576\n"
      "v_mfma_f32_16x16x32_bf16 v[28:31], v[84:87], a[116:119], v[28:31]\n"
      "s_add_u32 m0, s92, 18176\n"
      "v_add_f32 v115, v115, v63\n"
      "v_cvt_pk_bf16_f32 v59, v62, v63\n"
      "v_mfma_f32_16x16x32_bf16 v[28:31], v[80:83], a[112:115], v[28:31]\n"
      "s_waitcnt lgkmcnt(4)\n"
      "buffer_load_dwordx4 v119, s[20:23], s52 offen offset:256 nt lds\n"
      "v_mfma_f32_16x16x32_bf16 v[28:31], v[88:91], a[120:123], v[28:31]\n"
      "v_exp_f32 v0, v0\n"
      "v_mfma_f32_16x16x32_bf16 v[28:31], v[92:95], a[124:127], v[28:31]\n"
      "ds_read_b64_tr_b16 v[72:73], v117 offset:16896\n"
      "v_mfma_f32_16x16x32_bf16 v[20:23], v[92:95], a[92:95], v[20:23]\n"
      "ds_read_b64_tr_b16 v[74:75], v117 offset:25088\n"
      "v_add_f32 v112, v112, v0\n"
      "v_mfma_f32_16x16x32_bf16 v[20:23], v[88:91], a[88:91], v[20:23]\n"
      "ds_read_b64_tr_b16 v[76:77], v118 offset:16896\n"
      "v_mfma_f32_16x16x32_bf16 v[12:15], v[88:91], a[56:59], v[12:15]\n"
      "v_exp_f32 v1, v1\n"
      "v_mfma_f32_16x16x32_bf16 v[12:15], v[92:95], a[60:63], v[12:15]\n"
      "ds_read_b64_tr_b16 v[78:79], v118 offset:25088\n"
      "v_mfma_f32_16x16x32_bf16 v[4:7], v[92:95], a[28:31], v[4:7]\n"
      "s_add_u32 m0, s92, 19072\n"
      "v_add_f32 v112, v112, v1\n"
      "v_cvt_pk_bf16_f32 v0, v0, v1\n"
      "v_mfma_f32_16x16x32_bf16 v[4:7], v[88:91], a[24:27], v[4:7]\n"
      "s_waitcnt lgkmcnt(4)\n"
      "buffer_load_dwordx4 v119, s[20:23], s52 offen offset:384 nt lds\n"
      "v_mfma_f32_16x16x32_bf16 v[128:131], v[32:35], v[64:67], v[128:131]\n"
      "v_exp_f32 v2, v2\n"
      "v_mfma_f32_16x16x32_bf16 v[132:135], v[32:35], v[68:71], v[132:135]\n"
      "ds_read_b64_tr_b16 v[80:81], v117 offset:17408\n"
      "v_mfma_f32_16x16x32_bf16 v[196:199], v[40:43], v[68:71], v[196:199]\n"
      "ds_read_b64_tr_b16 v[82:83], v117 offset:25600\n"
      "v_add_f32 v112, v112, v2\n"
      "v_mfma_f32_16x16x32_bf16 v[192:195], v[40:43], v[64:67], v[192:195]\n"
      "ds_read_b64_tr_b16 v[84:85], v118 offset:17408\n"
      "v_mfma_f32_16x16x32_bf16 a[128:131], v[48:51], v[64:67], a[128:131]\n"
      "v_exp_f32 v3, v3\n"
      "v_mfma_f32_16x16x32_bf16 a[132:135], v[48:51], v[68:71], a[132:135]\n"
      "s_mov_b32 s52, 65536\n"
      "ds_read_b64_tr_b16 v[86:87], v118 offset:25600\n"
      "v_mfma_f32_16x16x32_bf16 a[196:199], v[56:59], v[68:71], a[196:199]\n"
      "s_add_u32 m0, s92, 65536\n"
      "v_add_f32 v112, v112, v3\n"
      "v_cvt_pk_bf16_f32 v1, v2, v3\n"
      "v_mfma_f32_16x16x32_bf16 a[192:195], v[56:59], v[64:67], a[192:195]\n"
      "s_waitcnt lgkmcnt(4)\n"
      "buffer_load_dwordx4 v120, s[24:27], s52 offen offset:0 nt lds\n"
      "v_mfma_f32_16x16x32_bf16 a[200:203], v[56:59], v[72:75], a[200:203]\n"
      "v_exp_f32 v4, v4\n"
      "v_mfma_f32_16x16x32_bf16 a[204:207], v[56:59], v[76:79], a[204:207]\n"
      "ds_read_b64_tr_b16 v[88:89], v117 offset:17920\n"
      "v_mfma_f32_16x16x32_bf16 a[140:143], v[48:51], v[76:79], a[140:143]\n"
      "ds_read_b64_tr_b16 v[90:91], v117 offset:26112\n"
      "v_add_f32 v112, v112, v4\n"
      "v_mfma_f32_16x16x32_bf16 a[136:139], v[48:51], v[72:75], a[136:139]\n"
      "ds_read_b64_tr_b16 v[92:93], v118 offset:17920\n"
      "v_mfma_f32_16x16x32_bf16 v[200:203], v[40:43], v[72:75], v[200:203]\n"
      "v_exp_f32 v5, v5\n"
      "v_mfma_f32_16x16x32_bf16 v[204:207], v[40:43], v[76:79], v[204:207]\n"
      "ds_read_b64_tr_b16 v[94:95], v118 offset:26112\n"
      "v_mfma_f32_16x16x32_bf16 v[140:143], v[32:35], v[76:79], v[140:143]\n"
      "s_add_u32 m0, s92, 66432\n"
      "v_add_f32 v112, v112, v5\n"
      "v_cvt_pk_bf16_f32 v2, v4, v5\n"
      "v_mfma_f32_16x16x32_bf16 v[136:139], v[32:35], v[72:75], v[136:139]\n"
      "s_waitcnt lgkmcnt(4)\n"
      "buffer_load_dwordx4 v120, s[24:27], s52 offen offset:128 nt lds\n"
      "v_mfma_f32_16x16x32_bf16 v[144:147], v[32:35], v[80:83], v[144:147]\n"
      "v_exp_f32 v6, v6\n"
      "v_mfma_f32_16x16x32_bf16 v[148:151], v[32:35], v[84:87], v[148:151]\n"
      "ds_read_b64_tr_b16 v[64:65], v117 offset:18432\n"
      "v_mfma_f32_16x16x32_bf16 v[212:215], v[40:43], v[84:87], v[212:215]\n"
      "ds_read_b64_tr_b16 v[66:67], v117 offset:26624\n"
      "v_add_f32 v112, v112, v6\n"
      "v_mfma_f32_16x16x32_bf16 v[208:211], v[40:43], v[80:83], v[208:211]\n"
      "ds_read_b64_tr_b16 v[68:69], v118 offset:18432\n"
      "v_mfma_f32_16x16x32_bf16 a[144:147], v[48:51], v[80:83], a[144:147]\n"
      "v_exp_f32 v7, v7\n"
      "v_mfma_f32_16x16x32_bf16 a[148:151], v[48:51], v[84:87], a[148:151]\n"
      "ds_read_b64_tr_b16 v[70:71], v118 offset:26624\n"
      "v_mfma_f32_16x16x32_bf16 a[212:215], v[56:59], v[84:87], a[212:215]\n"
      "s_add_u32 m0, s92, 67328\n"
      "v_add_f32 v112, v112, v7\n"
      "v_cvt_pk_bf16_f32 v3, v6, v7\n"
      "v_mfma_f32_16x16x32_bf16 a[208:211], v[56:59], v[80:83], a[208:211]\n"
      "s_waitcnt lgkmcnt(4)\n"
      "buffer_load_dwordx4 v120, s[24:27], s52 offen offset:256 nt lds\n"
      "v_mfma_f32_16x16x32_bf16 a[216:219], v[56:59], v[88:91], a[216:219]\n"
      "v_exp_f32 v8, v8\n"
      "v_mfma_f32_16x16x32_bf16 a[220:223], v[56:59], v[92:95], a[220:223]\n"
      "ds_read_b64_tr_b16 v[72:73], v117 offset:18944\n"
      "v_mfma_f32_16x16x32_bf16 a[156:159], v[48:51], v[92:95], a[156:159]\n"
      "ds_read_b64_tr_b16 v[74:75], v117 offset:27136\n"
      "v_add_f32 v113, v113, v8\n"
      "v_mfma_f32_16x16x32_bf16 a[152:155], v[48:51], v[88:91], a[152:155]\n"
      "ds_read_b64_tr_b16 v[76:77], v118 offset:18944\n"
      "v_mfma_f32_16x16x32_bf16 v[216:219], v[40:43], v[88:91], v[216:219]\n"
      "v_exp_f32 v9, v9\n"
      "v_mfma_f32_16x16x32_bf16 v[220:223], v[40:43], v[92:95], v[220:223]\n"
      "ds_read_b64_tr_b16 v[78:79], v118 offset:27136\n"
      "v_mfma_f32_16x16x32_bf16 v[156:159], v[32:35], v[92:95], v[156:159]\n"
      "s_add_u32 m0, s92, 68224\n"
      "v_add_f32 v113, v113, v9\n"
      "v_cvt_pk_bf16_f32 v8, v8, v9\n"
      "v_mfma_f32_16x16x32_bf16 v[152:155], v[32:35], v[88:91], v[152:155]\n"
      "s_waitcnt lgkmcnt(4)\n"
      "buffer_load_dwordx4 v120, s[24:27], s52 offen offset:384 nt lds\n"
      "v_mfma_f32_16x16x32_bf16 v[160:163], v[32:35], v[64:67], v[160:163]\n"
      "v_exp_f32 v10, v10\n"
      "v_mfma_f32_16x16x32_bf16 v[164:167], v[32:35], v[68:71], v[164:167]\n"
      "ds_read_b64_tr_b16 v[80:81], v117 offset:19456\n"
      "v_mfma_f32_16x16x32_bf16 v[228:231], v[40:43], v[68:71], v[228:231]\n"
      "ds_read_b64_tr_b16 v[82:83], v117 offset:27648\n"
      "v_add_f32 v113, v113, v10\n"
      "v_mfma_f32_16x16x32_bf16 v[224:227], v[40:43], v[64:67], v[224:227]\n"
      "ds_read_b64_tr_b16 v[84:85], v118 offset:19456\n"
      "v_mfma_f32_16x16x32_bf16 a[160:163], v[48:51], v[64:67], a[160:163]\n"
      "v_exp_f32 v11, v11\n"
      "v_mfma_f32_16x16x32_bf16 a[164:167], v[48:51], v[68:71], a[164:167]\n"
      "s_mov_b32 s52, 491520\n"
      "ds_read_b64_tr_b16 v[86:87], v118 offset:27648\n"
      "v_mfma_f32_16x16x32_bf16 a[228:231], v[56:59], v[68:71], a[228:231]\n"
      "s_add_u32 m0, s92, 131072\n"
      "v_add_f32 v113, v113, v11\n"
      "v_cvt_pk_bf16_f32 v9, v10, v11\n"
      "v_mfma_f32_16x16x32_bf16 a[224:227], v[56:59], v[64:67], a[224:227]\n"
      "s_waitcnt lgkmcnt(4)\n"
      "buffer_load_dwordx4 v120, s[24:27], s52 offen offset:0 nt lds\n"
      "v_mfma_f32_16x16x32_bf16 a[232:235], v[56:59], v[72:75], a[232:235]\n"
      "v_exp_f32 v12, v12\n"
      "v_mfma_f32_16x16x32_bf16 a[236:239], v[56:59], v[76:79], a[236:239]\n"
      "ds_read_b64_tr_b16 v[88:89], v117 offset:19968\n"
      "v_mfma_f32_16x16x32_bf16 a[172:175], v[48:51], v[76:79], a[172:175]\n"
      "ds_read_b64_tr_b16 v[90:91], v117 offset:28160\n"
      "v_add_f32 v113, v113, v12\n"
      "v_mfma_f32_16x16x32_bf16 a[168:171], v[48:51], v[72:75], a[168:171]\n"
      "ds_read_b64_tr_b16 v[92:93], v118 offset:19968\n"
      "v_mfma_f32_16x16x32_bf16 v[232:235], v[40:43], v[72:75], v[232:235]\n"
      "s_waitcnt vmcnt(20)\n"
      "s_barrier\n"
      "v_exp_f32 v13, v13\n"
      "v_mfma_f32_16x16x32_bf16 v[236:239], v[40:43], v[76:79], v[236:239]\n"
      "ds_read_b64_tr_b16 v[94:95], v118 offset:28160\n"
      "v_mfma_f32_16x16x32_bf16 v[172:175], v[32:35], v[76:79], v[172:175]\n"
      "s_add_u32 m0, s92, 131968\n"
      "v_add_f32 v113, v113, v13\n"
      "v_cvt_pk_bf16_f32 v10, v12, v13\n"
      "v_mfma_f32_16x16x32_bf16 v[168:171], v[32:35], v[72:75], v[168:171]\n"
      "s_waitcnt lgkmcnt(4)\n"
      "buffer_load_dwordx4 v120, s[24:27], s52 offen offset:128 nt lds\n"
      "v_mfma_f32_16x16x32_bf16 v[176:179], v[32:35], v[80:83], v[176:179]\n"
      "v_exp_f32 v14, v14\n"
      "v_mfma_f32_16x16x32_bf16 v[180:183], v[32:35], v[84:87], v[180:183]\n"
      "ds_read_b128 v[64:67], v116 offset:49152\n"
      "v_mfma_f32_16x16x32_bf16 v[244:247], v[40:43], v[84:87], v[244:247]\n"
      "v_add_f32 v113, v113, v14\n"
      "v_mfma_f32_16x16x32_bf16 v[240:243], v[40:43], v[80:83], v[240:243]\n"
      "ds_read_b128 v[68:71], v116 offset:49664\n"
      "v_mfma_f32_16x16x32_bf16 a[176:179], v[48:51], v[80:83], a[176:179]\n"
      "v_exp_f32 v15, v15\n"
      "v_mfma_f32_16x16x32_bf16 a[180:183], v[48:51], v[84:87], a[180:183]\n"
      "v_mfma_f32_16x16x32_bf16 a[244:247], v[56:59], v[84:87], a[244:247]\n"
      "s_add_u32 m0, s92, 132864\n"
      "v_add_f32 v113, v113, v15\n"
      "v_cvt_pk_bf16_f32 v11, v14, v15\n"
      "v_mfma_f32_16x16x32_bf16 a[240:243], v[56:59], v[80:83], a[240:243]\n"
      "s_waitcnt lgkmcnt(2)\n"
      "buffer_load_dwordx4 v120, s[24:27], s52 offen offset:256 nt lds\n"
      "v_mfma_f32_16x16x32_bf16 a[248:251], v[56:59], v[88:91], a[248:251]\n"
      "v_exp_f32 v16, v16\n"
      "v_mfma_f32_16x16x32_bf16 a[252:255], v[56:59], v[92:95], a[252:255]\n"
      "ds_read_b128 v[72:75], v116 offset:50176\n"
      "v_mfma_f32_16x16x32_bf16 a[188:191], v[48:51], v[92:95], a[188:191]\n"
      "v_add_f32 v114, v114, v16\n"
      "v_mfma_f32_16x16x32_bf16 a[184:187], v[48:51], v[88:91], a[184:187]\n"
      "ds_read_b128 v[76:79], v116 offset:50688\n"
      "v_mfma_f32_16x16x32_bf16 v[248:251], v[40:43], v[88:91], v[248:251]\n"
      "v_exp_f32 v17, v17\n"
      "v_mfma_f32_16x16x32_bf16 v[252:255], v[40:43], v[92:95], v[252:255]\n"
      "v_mfma_f32_16x16x32_bf16 v[188:191], v[32:35], v[92:95], v[188:191]\n"
      "s_add_u32 m0, s92, 133760\n"
      "v_add_f32 v114, v114, v17\n"
      "v_cvt_pk_bf16_f32 v16, v16, v17\n"
      "v_mfma_f32_16x16x32_bf16 v[184:187], v[32:35], v[88:91], v[184:187]\n"
      "s_waitcnt lgkmcnt(2)\n"
      "buffer_load_dwordx4 v120, s[24:27], s52 offen offset:384 nt lds\n"
      "LOOP%=:\n"
      "v_mfma_f32_16x16x32_bf16 v[32:35], v[64:67], a[0:3], v[96:99]\n"
      "v_exp_f32 v18, v18\n"
      "v_mfma_f32_16x16x32_bf16 v[32:35], v[68:71], a[4:7], v[32:35]\n"
      "s_waitcnt vmcnt(12)\n"
      "s_barrier\n"
      "ds_read_b128 v[80:83], v116 offset:51200\n"
      "v_mfma_f32_16x16x32_bf16 v[40:43], v[68:71], a[36:39], v[100:103]\n"
      "v_add_f32 v114, v114, v18\n"
      "v_mfma_f32_16x16x32_bf16 v[40:43], v[64:67], a[32:35], v[40:43]\n"
      "ds_read_b128 v[84:87], v116 offset:51712\n"
      "v_mfma_f32_16x16x32_bf16 v[48:51], v[64:67], a[64:67], v[104:107]\n"
      "v_exp_f32 v19, v19\n"
      "v_mfma_f32_16x16x32_bf16 v[48:51], v[68:71], a[68:71], v[48:51]\n"
      "v_mfma_f32_16x16x32_bf16 v[56:59], v[68:71], a[100:103], v[108:111]\n"
      "s_add_u32 m0, s92, 32768\n"
      "v_add_f32 v114, v114, v19\n"
      "v_cvt_pk_bf16_f32 v17, v18, v19\n"
      "v_mfma_f32_16x16x32_bf16 v[56:59], v[64:67], a[96:99], v[56:59]\n"
      "s_waitcnt lgkmcnt(2)\n"
      "buffer_load_dwordx4 v119, s[20:23], s36 offen offset:0 nt lds\n"
      "v_mfma_f32_16x16x32_bf16 v[56:59], v[72:75], a[104:107], v[56:59]\n"
      "v_exp_f32 v20, v20\n"
      "v_mfma_f32_16x16x32_bf16 v[56:59], v[76:79], a[108:111], v[56:59]\n"
      "ds_read_b128 v[88:91], v116 offset:52224\n"
      "v_mfma_f32_16x16x32_bf16 v[48:51], v[76:79], a[76:79], v[48:51]\n"
      "v_add_f32 v114, v114, v20\n"
      "v_mfma_f32_16x16x32_bf16 v[48:51], v[72:75], a[72:75], v[48:51]\n"
      "v_mfma_f32_16x16x32_bf16 v[40:43], v[72:75], a[40:43], v[40:43]\n"
      "v_exp_f32 v21, v21\n"
      "v_mfma_f32_16x16x32_bf16 v[40:43], v[76:79], a[44:47], v[40:43]\n"
      "ds_read_b128 v[92:95], v116 offset:52736\n"
      "v_mfma_f32_16x16x32_bf16 v[32:35], v[76:79], a[12:15], v[32:35]\n"
      "v_add_f32 v114, v114, v21\n"
      "v_mfma_f32_16x16x32_bf16 v[32:35], v[72:75], a[8:11], v[32:35]\n"
      "s_waitcnt lgkmcnt(2)\n"
      "v_cvt_pk_bf16_f32 v18, v20, v21\n"
      "v_mfma_f32_16x16x32_bf16 v[32:35], v[80:83], a[16:19], v[32:35]\n"
      "v_exp_f32 v22, v22\n"
      "v_mfma_f32_16x16x32_bf16 v[32:35], v[84:87], a[20:23], v[32:35]\n"
      "ds_read_b128 v[64:67], v116 offset:57344\n"
      "v_mfma_f32_16x16x32_bf16 v[40:43], v[84:87], a[52:55], v[40:43]\n"
      "v_add_f32 v114, v114, v22\n"
      "v_mfma_f32_16x16x32_bf16 v[40:43], v[80:83], a[48:51], v[40:43]\n"
      "ds_read_b128 v[68:71], v116 offset:57856\n"
      "v_mfma_f32_16x16x32_bf16 v[48:51], v[80:83], a[80:83], v[48:51]\n"
      "v_exp_f32 v23, v23\n"
      "v_mfma_f32_16x16x32_bf16 v[48:51], v[84:87], a[84:87], v[48:51]\n"
      "v_mfma_f32_16x16x32_bf16 v[56:59], v[84:87], a[116:119], v[56:59]\n"
      "s_add_u32 m0, s92, 33664\n"
      "v_add_f32 v114, v114, v23\n"
      "v_cvt_pk_bf16_f32 v19, v22, v23\n"
      "v_mfma_f32_16x16x32_bf16 v[56:59], v[80:83], a[112:115], v[56:59]\n"
      "s_waitcnt lgkmcnt(2)\n"
      "buffer_load_dwordx4 v119, s[20:23], s36 offen offset:128 nt lds\n"
      "v_mfma_f32_16x16x32_bf16 v[56:59], v[88:91], a[120:123], v[56:59]\n"
      "v_exp_f32 v24, v24\n"
      "v_mfma_f32_16x16x32_bf16 v[56:59], v[92:95], a[124:127], v[56:59]\n"
      "ds_read_b128 v[72:75], v116 offset:58368\n"
      "v_mfma_f32_16x16x32_bf16 v[48:51], v[92:95], a[92:95], v[48:51]\n"
      "v_add_f32 v115, v115, v24\n"
      "v_mfma_f32_16x16x32_bf16 v[48:51], v[88:91], a[88:91], v[48:51]\n"
      "v_mfma_f32_16x16x32_bf16 v[40:43], v[88:91], a[56:59], v[40:43]\n"
      "v_exp_f32 v25, v25\n"
      "v_mfma_f32_16x16x32_bf16 v[40:43], v[92:95], a[60:63], v[40:43]\n"
      "ds_read_b128 v[76:79], v116 offset:58880\n"
      "v_mfma_f32_16x16x32_bf16 v[32:35], v[92:95], a[28:31], v[32:35]\n"
      "v_add_f32 v115, v115, v25\n"
      "v_mfma_f32_16x16x32_bf16 v[32:35], v[88:91], a[24:27], v[32:35]\n"
      "s_waitcnt lgkmcnt(2)\n"
      "v_cvt_pk_bf16_f32 v24, v24, v25\n"
      "v_mfma_f32_16x16x32_bf16 v[36:39], v[64:67], a[0:3], v[96:99]\n"
      "v_exp_f32 v26, v26\n"
      "v_mfma_f32_16x16x32_bf16 v[36:39], v[68:71], a[4:7], v[36:39]\n"
      "ds_read_b128 v[80:83], v116 offset:59392\n"
      "v_mfma_f32_16x16x32_bf16 v[44:47], v[68:71], a[36:39], v[100:103]\n"
      "v_add_f32 v115, v115, v26\n"
      "v_mfma_f32_16x16x32_bf16 v[44:47], v[64:67], a[32:35], v[44:47]\n"
      "ds_read_b128 v[84:87], v116 offset:59904\n"
      "v_mfma_f32_16x16x32_bf16 v[52:55], v[64:67], a[64:67], v[104:107]\n"
      "v_exp_f32 v27, v27\n"
      "v_mfma_f32_16x16x32_bf16 v[52:55], v[68:71], a[68:71], v[52:55]\n"
      "v_mfma_f32_16x16x32_bf16 v[60:63], v[68:71], a[100:103], v[108:111]\n"
      "s_add_u32 m0, s92, 34560\n"
      "v_add_f32 v115, v115, v27\n"
      "v_cvt_pk_bf16_f32 v25, v26, v27\n"
      "v_mfma_f32_16x16x32_bf16 v[60:63], v[64:67], a[96:99], v[60:63]\n"
      "s_waitcnt lgkmcnt(2)\n"
      "buffer_load_dwordx4 v119, s[20:23], s36 offen offset:256 nt lds\n"
      "v_mfma_f32_16x16x32_bf16 v[60:63], v[72:75], a[104:107], v[60:63]\n"
      "v_exp_f32 v28, v28\n"
      "v_mfma_f32_16x16x32_bf16 v[60:63], v[76:79], a[108:111], v[60:63]\n"
      "ds_read_b128 v[88:91], v116 offset:60416\n"
      "v_mfma_f32_16x16x32_bf16 v[52:55], v[76:79], a[76:79], v[52:55]\n"
      "v_add_f32 v115, v115, v28\n"
      "v_mfma_f32_16x16x32_bf16 v[52:55], v[72:75], a[72:75], v[52:55]\n"
      "v_mfma_f32_16x16x32_bf16 v[44:47], v[72:75], a[40:43], v[44:47]\n"
      "v_exp_f32 v29, v29\n"
      "v_mfma_f32_16x16x32_bf16 v[44:47], v[76:79], a[44:47], v[44:47]\n"
      "ds_read_b128 v[92:95], v116 offset:60928\n"
      "v_mfma_f32_16x16x32_bf16 v[36:39], v[76:79], a[12:15], v[36:39]\n"
      "v_add_f32 v115, v115, v29\n"
      "v_mfma_f32_16x16x32_bf16 v[36:39], v[72:75], a[8:11], v[36:39]\n"
      "s_waitcnt lgkmcnt(2)\n"
      "v_cvt_pk_bf16_f32 v26, v28, v29\n"
      "v_mfma_f32_16x16x32_bf16 v[36:39], v[80:83], a[16:19], v[36:39]\n"
      "v_exp_f32 v30, v30\n"
      "v_mfma_f32_16x16x32_bf16 v[36:39], v[84:87], a[20:23], v[36:39]\n"
      "ds_read_b64_tr_b16 v[64:65], v117 offset:32768\n"
      "v_mfma_f32_16x16x32_bf16 v[44:47], v[84:87], a[52:55], v[44:47]\n"
      "ds_read_b64_tr_b16 v[66:67], v117 offset:40960\n"
      "v_add_f32 v115, v115, v30\n"
      "v_mfma_f32_16x16x32_bf16 v[44:47], v[80:83], a[48:51], v[44:47]\n"
      "ds_read_b64_tr_b16 v[68:69], v118 offset:32768\n"
      "v_mfma_f32_16x16x32_bf16 v[52:55], v[80:83], a[80:83], v[52:55]\n"
      "v_exp_f32 v31, v31\n"
      "v_mfma_f32_16x16x32_bf16 v[52:55], v[84:87], a[84:87], v[52:55]\n"
      "ds_read_b64_tr_b16 v[70:71], v118 offset:40960\n"
      "v_mfma_f32_16x16x32_bf16 v[60:63], v[84:87], a[116:119], v[60:63]\n"
      "s_add_u32 m0, s92, 35456\n"
      "v_add_f32 v115, v115, v31\n"
      "v_cvt_pk_bf16_f32 v27, v30, v31\n"
      "v_mfma_f32_16x16x32_bf16 v[60:63], v[80:83], a[112:115], v[60:63]\n"
      "s_waitcnt lgkmcnt(4)\n"
      "buffer_load_dwordx4 v119, s[20:23], s36 offen offset:384 nt lds\n"
      "v_mfma_f32_16x16x32_bf16 v[60:63], v[88:91], a[120:123], v[60:63]\n"
      "v_exp_f32 v32, v32\n"
      "v_mfma_f32_16x16x32_bf16 v[60:63], v[92:95], a[124:127], v[60:63]\n"
      "ds_read_b64_tr_b16 v[72:73], v117 offset:33280\n"
      "v_mfma_f32_16x16x32_bf16 v[52:55], v[92:95], a[92:95], v[52:55]\n"
      "v_add_f32 v112, v112, v32\n"
      "v_mfma_f32_16x16x32_bf16 v[52:55], v[88:91], a[88:91], v[52:55]\n"
      "ds_read_b64_tr_b16 v[74:75], v117 offset:41472\n"
      "v_mfma_f32_16x16x32_bf16 v[44:47], v[88:91], a[56:59], v[44:47]\n"
      "v_exp_f32 v33, v33\n"
      "v_mfma_f32_16x16x32_bf16 v[44:47], v[92:95], a[60:63], v[44:47]\n"
      "ds_read_b64_tr_b16 v[76:77], v118 offset:33280\n"
      "v_mfma_f32_16x16x32_bf16 v[36:39], v[92:95], a[28:31], v[36:39]\n"
      "v_add_f32 v112, v112, v33\n"
      "v_mfma_f32_16x16x32_bf16 v[36:39], v[88:91], a[24:27], v[36:39]\n"
      "s_waitcnt lgkmcnt(3)\n"
      "ds_read_b64_tr_b16 v[78:79], v118 offset:41472\n"
      "v_cvt_pk_bf16_f32 v32, v32, v33\n"
      "v_mfma_f32_16x16x32_bf16 v[128:131], v[0:3], v[64:67], v[128:131]\n"
      "v_exp_f32 v34, v34\n"
      "v_mfma_f32_16x16x32_bf16 v[132:135], v[0:3], v[68:71], v[132:135]\n"
      "ds_read_b64_tr_b16 v[80:81], v117 offset:33792\n"
      "v_mfma_f32_16x16x32_bf16 v[196:199], v[8:11], v[68:71], v[196:199]\n"
      "ds_read_b64_tr_b16 v[82:83], v117 offset:41984\n"
      "v_add_f32 v112, v112, v34\n"
      "v_mfma_f32_16x16x32_bf16 v[192:195], v[8:11], v[64:67], v[192:195]\n"
      "ds_read_b64_tr_b16 v[84:85], v118 offset:33792\n"
      "v_mfma_f32_16x16x32_bf16 a[128:131], v[16:19], v[64:67], a[128:131]\n"
      "v_exp_f32 v35, v35\n"
      "v_mfma_f32_16x16x32_bf16 a[132:135], v[16:19], v[68:71], a[132:135]\n"
      "ds_read_b64_tr_b16 v[86:87], v118 offset:41984\n"
      "v_mfma_f32_16x16x32_bf16 a[196:199], v[24:27], v[68:71], a[196:199]\n"
      "s_add_u32 m0, s92, 81920\n"
      "v_add_f32 v112, v112, v35\n"
      "v_cvt_pk_bf16_f32 v33, v34, v35\n"
      "v_mfma_f32_16x16x32_bf16 a[192:195], v[24:27], v[64:67], a[192:195]\n"
      "s_waitcnt lgkmcnt(4)\n"
      "buffer_load_dwordx4 v120, s[24:27], s37 offen offset:0 nt lds\n"
      "v_mfma_f32_16x16x32_bf16 a[200:203], v[24:27], v[72:75], a[200:203]\n"
      "v_exp_f32 v36, v36\n"
      "v_mfma_f32_16x16x32_bf16 a[204:207], v[24:27], v[76:79], a[204:207]\n"
      "ds_read_b64_tr_b16 v[88:89], v117 offset:34304\n"
      "v_mfma_f32_16x16x32_bf16 a[140:143], v[16:19], v[76:79], a[140:143]\n"
      "v_add_f32 v112, v112, v36\n"
      "v_mfma_f32_16x16x32_bf16 a[136:139], v[16:19], v[72:75], a[136:139]\n"
      "s_add_u32 s36, s36, 16384\n"
      "ds_read_b64_tr_b16 v[90:91], v117 offset:42496\n"
      "v_mfma_f32_16x16x32_bf16 v[200:203], v[8:11], v[72:75], v[200:203]\n"
      "v_exp_f32 v37, v37\n"
      "v_mfma_f32_16x16x32_bf16 v[204:207], v[8:11], v[76:79], v[204:207]\n"
      "ds_read_b64_tr_b16 v[92:93], v118 offset:34304\n"
      "v_mfma_f32_16x16x32_bf16 v[140:143], v[0:3], v[76:79], v[140:143]\n"
      "v_add_f32 v112, v112, v37\n"
      "v_mfma_f32_16x16x32_bf16 v[136:139], v[0:3], v[72:75], v[136:139]\n"
      "s_waitcnt lgkmcnt(3)\n"
      "ds_read_b64_tr_b16 v[94:95], v118 offset:42496\n"
      "v_cvt_pk_bf16_f32 v34, v36, v37\n"
      "v_mfma_f32_16x16x32_bf16 v[144:147], v[0:3], v[80:83], v[144:147]\n"
      "v_exp_f32 v38, v38\n"
      "v_mfma_f32_16x16x32_bf16 v[148:151], v[0:3], v[84:87], v[148:151]\n"
      "ds_read_b64_tr_b16 v[64:65], v117 offset:34816\n"
      "v_mfma_f32_16x16x32_bf16 v[212:215], v[8:11], v[84:87], v[212:215]\n"
      "ds_read_b64_tr_b16 v[66:67], v117 offset:43008\n"
      "v_add_f32 v112, v112, v38\n"
      "v_mfma_f32_16x16x32_bf16 v[208:211], v[8:11], v[80:83], v[208:211]\n"
      "ds_read_b64_tr_b16 v[68:69], v118 offset:34816\n"
      "v_mfma_f32_16x16x32_bf16 a[144:147], v[16:19], v[80:83], a[144:147]\n"
      "v_exp_f32 v39, v39\n"
      "v_mfma_f32_16x16x32_bf16 a[148:151], v[16:19], v[84:87], a[148:151]\n"
      "ds_read_b64_tr_b16 v[70:71], v118 offset:43008\n"
      "v_mfma_f32_16x16x32_bf16 a[212:215], v[24:27], v[84:87], a[212:215]\n"
      "s_add_u32 m0, s92, 82816\n"
      "v_add_f32 v112, v112, v39\n"
      "v_cvt_pk_bf16_f32 v35, v38, v39\n"
      "v_mfma_f32_16x16x32_bf16 a[208:211], v[24:27], v[80:83], a[208:211]\n"
      "s_waitcnt lgkmcnt(4)\n"
      "buffer_load_dwordx4 v120, s[24:27], s37 offen offset:128 nt lds\n"
      "v_mfma_f32_16x16x32_bf16 a[216:219], v[24:27], v[88:91], a[216:219]\n"
      "v_exp_f32 v40, v40\n"
      "v_mfma_f32_16x16x32_bf16 a[220:223], v[24:27], v[92:95], a[220:223]\n"
      "ds_read_b64_tr_b16 v[72:73], v117 offset:35328\n"
      "v_mfma_f32_16x16x32_bf16 a[156:159], v[16:19], v[92:95], a[156:159]\n"
      "v_add_f32 v113, v113, v40\n"
      "v_mfma_f32_16x16x32_bf16 a[152:155], v[16:19], v[88:91], a[152:155]\n"
      "ds_read_b64_tr_b16 v[74:75], v117 offset:43520\n"
      "v_mfma_f32_16x16x32_bf16 v[216:219], v[8:11], v[88:91], v[216:219]\n"
      "v_exp_f32 v41, v41\n"
      "v_mfma_f32_16x16x32_bf16 v[220:223], v[8:11], v[92:95], v[220:223]\n"
      "ds_read_b64_tr_b16 v[76:77], v118 offset:35328\n"
      "v_mfma_f32_16x16x32_bf16 v[156:159], v[0:3], v[92:95], v[156:159]\n"
      "v_add_f32 v113, v113, v41\n"
      "v_mfma_f32_16x16x32_bf16 v[152:155], v[0:3], v[88:91], v[152:155]\n"
      "s_waitcnt lgkmcnt(3)\n"
      "ds_read_b64_tr_b16 v[78:79], v118 offset:43520\n"
      "v_cvt_pk_bf16_f32 v40, v40, v41\n"
      "v_mfma_f32_16x16x32_bf16 v[160:163], v[0:3], v[64:67], v[160:163]\n"
      "v_exp_f32 v42, v42\n"
      "v_mfma_f32_16x16x32_bf16 v[164:167], v[0:3], v[68:71], v[164:167]\n"
      "ds_read_b64_tr_b16 v[80:81], v117 offset:35840\n"
      "v_mfma_f32_16x16x32_bf16 v[228:231], v[8:11], v[68:71], v[228:231]\n"
      "ds_read_b64_tr_b16 v[82:83], v117 offset:44032\n"
      "v_add_f32 v113, v113, v42\n"
      "v_mfma_f32_16x16x32_bf16 v[224:227], v[8:11], v[64:67], v[224:227]\n"
      "ds_read_b64_tr_b16 v[84:85], v118 offset:35840\n"
      "v_mfma_f32_16x16x32_bf16 a[160:163], v[16:19], v[64:67], a[160:163]\n"
      "v_exp_f32 v43, v43\n"
      "v_mfma_f32_16x16x32_bf16 a[164:167], v[16:19], v[68:71], a[164:167]\n"
      "ds_read_b64_tr_b16 v[86:87], v118 offset:44032\n"
      "v_mfma_f32_16x16x32_bf16 a[228:231], v[24:27], v[68:71], a[228:231]\n"
      "s_add_u32 m0, s92, 83712\n"
      "v_add_f32 v113, v113, v43\n"
      "v_cvt_pk_bf16_f32 v41, v42, v43\n"
      "v_mfma_f32_16x16x32_bf16 a[224:227], v[24:27], v[64:67], a[224:227]\n"
      "s_waitcnt lgkmcnt(4)\n"
      "buffer_load_dwordx4 v120, s[24:27], s37 offen offset:256 nt lds\n"
      "v_mfma_f32_16x16x32_bf16 a[232:235], v[24:27], v[72:75], a[232:235]\n"
      "v_exp_f32 v44, v44\n"
      "v_mfma_f32_16x16x32_bf16 a[236:239], v[24:27], v[76:79], a[236:239]\n"
      "ds_read_b64_tr_b16 v[88:89], v117 offset:36352\n"
      "v_mfma_f32_16x16x32_bf16 a[172:175], v[16:19], v[76:79], a[172:175]\n"
      "v_add_f32 v113, v113, v44\n"
      "v_mfma_f32_16x16x32_bf16 a[168:171], v[16:19], v[72:75], a[168:171]\n"
      "ds_read_b64_tr_b16 v[90:91], v117 offset:44544\n"
      "v_mfma_f32_16x16x32_bf16 v[232:235], v[8:11], v[72:75], v[232:235]\n"
      "v_exp_f32 v45, v45\n"
      "v_mfma_f32_16x16x32_bf16 v[236:239], v[8:11], v[76:79], v[236:239]\n"
      "ds_read_b64_tr_b16 v[92:93], v118 offset:36352\n"
      "v_mfma_f32_16x16x32_bf16 v[172:175], v[0:3], v[76:79], v[172:175]\n"
      "v_add_f32 v113, v113, v45\n"
      "v_mfma_f32_16x16x32_bf16 v[168:171], v[0:3], v[72:75], v[168:171]\n"
      "s_waitcnt lgkmcnt(3)\n"
      "ds_read_b64_tr_b16 v[94:95], v118 offset:44544\n"
      "v_cvt_pk_bf16_f32 v42, v44, v45\n"
      "v_mfma_f32_16x16x32_bf16 v[176:179], v[0:3], v[80:83], v[176:179]\n"
      "v_exp_f32 v46, v46\n"
      "v_mfma_f32_16x16x32_bf16 v[180:183], v[0:3], v[84:87], v[180:183]\n"
      "ds_read_b128 v[64:67], v116 offset:0\n"
      "v_mfma_f32_16x16x32_bf16 v[244:247], v[8:11], v[84:87], v[244:247]\n"
      "v_add_f32 v113, v113, v46\n"
      "v_mfma_f32_16x16x32_bf16 v[240:243], v[8:11], v[80:83], v[240:243]\n"
      "ds_read_b128 v[68:71], v116 offset:512\n"
      "v_mfma_f32_16x16x32_bf16 a[176:179], v[16:19], v[80:83], a[176:179]\n"
      "v_exp_f32 v47, v47\n"
      "v_mfma_f32_16x16x32_bf16 a[180:183], v[16:19], v[84:87], a[180:183]\n"
      "v_mfma_f32_16x16x32_bf16 a[244:247], v[24:27], v[84:87], a[244:247]\n"
      "s_add_u32 m0, s92, 84608\n"
      "v_add_f32 v113, v113, v47\n"
      "v_cvt_pk_bf16_f32 v43, v46, v47\n"
      "v_mfma_f32_16x16x32_bf16 a[240:243], v[24:27], v[80:83], a[240:243]\n"
      "s_waitcnt lgkmcnt(2)\n"
      "buffer_load_dwordx4 v120, s[24:27], s37 offen offset:384 nt lds\n"
      "v_mfma_f32_16x16x32_bf16 a[248:251], v[24:27], v[88:91], a[248:251]\n"
      "v_exp_f32 v48, v48\n"
      "v_mfma_f32_16x16x32_bf16 a[252:255], v[24:27], v[92:95], a[252:255]\n"
      "s_add_u32 s37, s37, 16384\n"
      "ds_read_b128 v[72:75], v116 offset:1024\n"
      "v_mfma_f32_16x16x32_bf16 a[188:191], v[16:19], v[92:95], a[188:191]\n"
      "v_add_f32 v114, v114, v48\n"
      "v_mfma_f32_16x16x32_bf16 a[184:187], v[16:19], v[88:91], a[184:187]\n"
      "v_mfma_f32_16x16x32_bf16 v[248:251], v[8:11], v[88:91], v[248:251]\n"
      "v_exp_f32 v49, v49\n"
      "v_mfma_f32_16x16x32_bf16 v[252:255], v[8:11], v[92:95], v[252:255]\n"
      "ds_read_b128 v[76:79], v116 offset:1536\n"
      "v_mfma_f32_16x16x32_bf16 v[188:191], v[0:3], v[92:95], v[188:191]\n"
      "v_add_f32 v114, v114, v49\n"
      "v_mfma_f32_16x16x32_bf16 v[184:187], v[0:3], v[88:91], v[184:187]\n"
      "s_waitcnt lgkmcnt(2)\n"
      "v_cvt_pk_bf16_f32 v48, v48, v49\n"
      "v_mfma_f32_16x16x32_bf16 v[0:3], v[64:67], a[0:3], v[96:99]\n"
      "v_exp_f32 v50, v50\n"
      "v_mfma_f32_16x16x32_bf16 v[0:3], v[68:71], a[4:7], v[0:3]\n"
      "s_waitcnt vmcnt(12)\n"
      "s_barrier\n"
      "ds_read_b128 v[80:83], v116 offset:2048\n"
      "v_mfma_f32_16x16x32_bf16 v[8:11], v[68:71], a[36:39], v[100:103]\n"
      "v_add_f32 v114, v114, v50\n"
      "v_mfma_f32_16x16x32_bf16 v[8:11], v[64:67], a[32:35], v[8:11]\n"
      "ds_read_b128 v[84:87], v116 offset:2560\n"
      "v_mfma_f32_16x16x32_bf16 v[16:19], v[64:67], a[64:67], v[104:107]\n"
      "v_exp_f32 v51, v51\n"
      "v_mfma_f32_16x16x32_bf16 v[16:19], v[68:71], a[68:71], v[16:19]\n"
      "v_mfma_f32_16x16x32_bf16 v[24:27], v[68:71], a[100:103], v[108:111]\n"
      "s_add_u32 m0, s92, 49152\n"
      "v_add_f32 v114, v114, v51\n"
      "v_cvt_pk_bf16_f32 v49, v50, v51\n"
      "v_mfma_f32_16x16x32_bf16 v[24:27], v[64:67], a[96:99], v[24:27]\n"
      "s_waitcnt lgkmcnt(2)\n"
      "buffer_load_dwordx4 v119, s[20:23], s36 offen offset:0 nt lds\n"
      "v_mfma_f32_16x16x32_bf16 v[24:27], v[72:75], a[104:107], v[24:27]\n"
      "v_exp_f32 v52, v52\n"
      "v_mfma_f32_16x16x32_bf16 v[24:27], v[76:79], a[108:111], v[24:27]\n"
      "ds_read_b128 v[88:91], v116 offset:3072\n"
      "v_mfma_f32_16x16x32_bf16 v[16:19], v[76:79], a[76:79], v[16:19]\n"
      "v_add_f32 v114, v114, v52\n"
      "v_mfma_f32_16x16x32_bf16 v[16:19], v[72:75], a[72:75], v[16:19]\n"
      "s_cmp_eq_u32 s39, 1\n"
      "s_cbranch_scc1 LATE4_%=\n"
      "BACK4_%=:\n"
      "v_mfma_f32_16x16x32_bf16 v[8:11], v[72:75], a[40:43], v[8:11]\n"
      "v_exp_f32 v53, v53\n"
      "v_mfma_f32_16x16x32_bf16 v[8:11], v[76:79], a[44:47], v[8:11]\n"
      "ds_read_b128 v[92:95], v116 offset:3584\n"
      "v_mfma_f32_16x16x32_bf16 v[0:3], v[76:79], a[12:15], v[0:3]\n"
      "v_add_f32 v114, v114, v53\n"
      "v_mfma_f32_16x16x32_bf16 v[0:3], v[72:75], a[8:11], v[0:3]\n"
      "s_waitcnt lgkmcnt(2)\n"
      "v_cvt_pk_bf16_f32 v50, v52, v53\n"
      "v_mfma_f32_16x16x32_bf16 v[0:3], v[80:83], a[16:19], v[0:3]\n"
      "v_exp_f32 v54, v54\n"
      "v_mfma_f32_16x16x32_bf16 v[0:3], v[84:87], a[20:23], v[0:3]\n"
      "ds_read_b128 v[64:67], v116 offset:8192\n"
      "v_mfma_f32_16x16x32_bf16 v[8:11], v[84:87], a[52:55], v[8:11]\n"
      "v_add_f32 v114, v114, v54\n"
      "v_mfma_f32_16x16x32_bf16 v[8:11], v[80:83], a[48:51], v[8:11]\n"
      "ds_read_b128 v[68:71], v116 offset:8704\n"
      "v_mfma_f32_16x16x32_bf16 v[16:19], v[80:83], a[80:83], v[16:19]\n"
      "v_exp_f32 v55, v55\n"
      "v_mfma_f32_16x16x32_bf16 v[16:19], v[84:87], a[84:87], v[16:19]\n"
      "v_mfma_f32_16x16x32_bf16 v[24:27], v[84:87], a[116:119], v[24:27]\n"
      "s_add_u32 m0, s92, 50048\n"
      "v_add_f32 v114, v114, v55\n"
      "v_cvt_pk_bf16_f32 v51, v54, v55\n"
      "v_mfma_f32_16x16x32_bf16 v[24:27], v[80:83], a[112:115], v[24:27]\n"
      "s_waitcnt lgkmcnt(2)\n"
      "buffer_load_dwordx4 v119, s[20:23], s36 offen offset:128 nt lds\n"
      "v_mfma_f32_16x16x32_bf16 v[24:27], v[88:91], a[120:123], v[24:27]\n"
      "v_exp_f32 v56, v56\n"
      "v_mfma_f32_16x16x32_bf16 v[24:27], v[92:95], a[124:127], v[24:27]\n"
      "ds_read_b128 v[72:75], v116 offset:9216\n"
      "v_mfma_f32_16x16x32_bf16 v[16:19], v[92:95], a[92:95], v[16:19]\n"
      "v_add_f32 v115, v115, v56\n"
      "v_mfma_f32_16x16x32_bf16 v[16:19], v[88:91], a[88:91], v[16:19]\n"
      "v_mfma_f32_16x16x32_bf16 v[8:11], v[88:91], a[56:59], v[8:11]\n"
      "v_exp_f32 v57, v57\n"
      "v_mfma_f32_16x16x32_bf16 v[8:11], v[92:95], a[60:63], v[8:11]\n"
      "ds_read_b128 v[76:79], v116 offset:9728\n"
      "v_mfma_f32_16x16x32_bf16 v[0:3], v[92:95], a[28:31], v[0:3]\n"
      "v_add_f32 v115, v115, v57\n"
      "v_mfma_f32_16x16x32_bf16 v[0:3], v[88:91], a[24:27], v[0:3]\n"
      "s_waitcnt lgkmcnt(2)\n"
      "v_cvt_pk_bf16_f32 v56, v56, v57\n"
      "v_mfma_f32_16x16x32_bf16 v[4:7], v[64:67], a[0:3], v[96:99]\n"
      "v_exp_f32 v58, v58\n"
      "v_mfma_f32_16x16x32_bf16 v[4:7], v[68:71], a[4:7], v[4:7]\n"
      "ds_read_b128 v[80:83], v116 offset:10240\n"
      "v_mfma_f32_16x16x32_bf16 v[12:15], v[68:71], a[36:39], v[100:103]\n"
      "v_add_f32 v115, v115, v58\n"
      "v_mfma_f32_16x16x32_bf16 v[12:15], v[64:67], a[32:35], v[12:15]\n"
      "ds_read_b128 v[84:87], v116 offset:10752\n"
      "v_mfma_f32_16x16x32_bf16 v[20:23], v[64:67], a[64:67], v[104:107]\n"
      "v_exp_f32 v59, v59\n"
      "v_mfma_f32_16x16x32_bf16 v[20:23], v[68:71], a[68:71], v[20:23]\n"
      "v_mfma_f32_16x16x32_bf16 v[28:31], v[68:71], a[100:103], v[108:111]\n"
      "s_add_u32 m0, s92, 50944\n"
      "v_add_f32 v115, v115, v59\n"
      "v_cvt_pk_bf16_f32 v57, v58, v59\n"
      "v_mfma_f32_16x16x32_bf16 v[28:31], v[64:67], a[96:99], v[28:31]\n"
      "s_waitcnt lgkmcnt(2)\n"
      "buffer_load_dwordx4 v119, s[20:23], s36 offen offset:256 nt lds\n"
      "v_mfma_f32_16x16x32_bf16 v[28:31], v[72:75], a[104:107], v[28:31]\n"
      "v_exp_f32 v60, v60\n"
      "v_mfma_f32_16x16x32_bf16 v[28:31], v[76:79], a[108:111], v[28:31]\n"
      "ds_read_b128 v[88:91], v116 offset:11264\n"
      "v_mfma_f32_16x16x32_bf16 v[20:23], v[76:79], a[76:79], v[20:23]\n"
      "v_add_f32 v115, v115, v60\n"
      "v_mfma_f32_16x16x32_bf16 v[20:23], v[72:75], a[72:75], v[20:23]\n"
      "v_mfma_f32_16x16x32_bf16 v[12:15], v[72:75], a[40:43], v[12:15]\n"
      "v_exp_f32 v61, v61\n"
      "v_mfma_f32_16x16x32_bf16 v[12:15], v[76:79], a[44:47], v[12:15]\n"
      "ds_read_b128 v[92:95], v116 offset:11776\n"
      "v_mfma_f32_16x16x32_bf16 v[4:7], v[76:79], a[12:15], v[4:7]\n"
      "v_add_f32 v115, v115, v61\n"
      "v_mfma_f32_16x16x32_bf16 v[4:7], v[72:75], a[8:11], v[4:7]\n"
      "s_waitcnt lgkmcnt(2)\n"
      "v_cvt_pk_bf16_f32 v58, v60, v61\n"
      "v_mfma_f32_16x16x32_bf16 v[4:7], v[80:83], a[16:19], v[4:7]\n"
      "v_exp_f32 v62, v62\n"
      "v_mfma_f32_16x16x32_bf16 v[4:7], v[84:87], a[20:23], v[4:7]\n"
      "ds_read_b64_tr_b16 v[64:65], v117 offset:49152\n"
      "v_mfma_f32_16x16x32_bf16 v[12:15], v[84:87], a[52:55], v[12:15]\n"
      "ds_read_b64_tr_b16 v[66:67], v117 offset:57344\n"
      "v_add_f32 v115, v115, v62\n"
      "v_mfma_f32_16x16x32_bf16 v[12:15], v[80:83], a[48:51], v[12:15]\n"
      "ds_read_b64_tr_b16 v[68:69], v118 offset:49152\n"
      "v_mfma_f32_16x16x32_bf16 v[20:23], v[80:83], a[80:83], v[20:23]\n"
      "v_exp_f32 v63, v63\n"
      "v_mfma_f32_16x16x32_bf16 v[20:23], v[84:87], a[84:87], v[20:23]\n"
      "ds_read_b64_tr_b16 v[70:71], v118 offset:57344\n"
      "v_mfma_f32_16x16x32_bf16 v[28:31], v[84:87], a[116:119], v[28:31]\n"
      "s_add_u32 m0, s92, 51840\n"
      "v_add_f32 v115, v115, v63\n"
      "v_cvt_pk_bf16_f32 v59, v62, v63\n"
      "v_mfma_f32_16x16x32_bf16 v[28:31], v[80:83], a[112:115], v[28:31]\n"
      "s_waitcnt lgkmcnt(4)\n"
      "buffer_load_dwordx4 v119, s[20:23], s36 offen offset:384 nt lds\n"
      "v_mfma_f32_16x16x32_bf16 v[28:31], v[88:91], a[120:123], v[28:31]\n"
      "v_exp_f32 v0, v0\n"
      "v_mfma_f32_16x16x32_bf16 v[28:31], v[92:95], a[124:127], v[28:31]\n"
      "ds_read_b64_tr_b16 v[72:73], v117 offset:49664\n"
      "v_mfma_f32_16x16x32_bf16 v[20:23], v[92:95], a[92:95], v[20:23]\n"
      "v_add_f32 v112, v112, v0\n"
      "v_mfma_f32_16x16x32_bf16 v[20:23], v[88:91], a[88:91], v[20:23]\n"
      "ds_read_b64_tr_b16 v[74:75], v117 offset:57856\n"
      "v_mfma_f32_16x16x32_bf16 v[12:15], v[88:91], a[56:59], v[12:15]\n"
      "v_exp_f32 v1, v1\n"
      "v_mfma_f32_16x16x32_bf16 v[12:15], v[92:95], a[60:63], v[12:15]\n"
      "ds_read_b64_tr_b16 v[76:77], v118 offset:49664\n"
      "v_mfma_f32_16x16x32_bf16 v[4:7], v[92:95], a[28:31], v[4:7]\n"
      "v_add_f32 v112, v112, v1\n"
      "v_mfma_f32_16x16x32_bf16 v[4:7], v[88:91], a[24:27], v[4:7]\n"
      "s_waitcnt lgkmcnt(3)\n"
      "ds_read_b64_tr_b16 v[78:79], v118 offset:57856\n"
      "v_cvt_pk_bf16_f32 v0, v0, v1\n"
      "v_mfma_f32_16x16x32_bf16 v[128:131], v[32:35], v[64:67], v[128:131]\n"
      "v_exp_f32 v2, v2\n"
      "v_mfma_f32_16x16x32_bf16 v[132:135], v[32:35], v[68:71], v[132:135]\n"
      "ds_read_b64_tr_b16 v[80:81], v117 offset:50176\n"
      "v_mfma_f32_16x16x32_bf16 v[196:199], v[40:43], v[68:71], v[196:199]\n"
      "ds_read_b64_tr_b16 v[82:83], v117 offset:58368\n"
      "v_add_f32 v112, v112, v2\n"
      "v_mfma_f32_16x16x32_bf16 v[192:195], v[40:43], v[64:67], v[192:195]\n"
      "ds_read_b64_tr_b16 v[84:85], v118 offset:50176\n"
      "v_mfma_f32_16x16x32_bf16 a[128:131], v[48:51], v[64:67], a[128:131]\n"
      "v_exp_f32 v3, v3\n"
      "v_mfma_f32_16x16x32_bf16 a[132:135], v[48:51], v[68:71], a[132:135]\n"
      "ds_read_b64_tr_b16 v[86:87], v118 offset:58368\n"
      "v_mfma_f32_16x16x32_bf16 a[196:199], v[56:59], v[68:71], a[196:199]\n"
      "s_add_u32 m0, s92, 98304\n"
      "v_add_f32 v112, v112, v3\n"
      "v_cvt_pk_bf16_f32 v1, v2, v3\n"
      "v_mfma_f32_16x16x32_bf16 a[192:195], v[56:59], v[64:67], a[192:195]\n"
      "s_waitcnt lgkmcnt(4)\n"
      "buffer_load_dwordx4 v120, s[24:27], s37 offen offset:0 nt lds\n"
      "v_mfma_f32_16x16x32_bf16 a[200:203], v[56:59], v[72:75], a[200:203]\n"
      "v_exp_f32 v4, v4\n"
      "v_mfma_f32_16x16x32_bf16 a[204:207], v[56:59], v[76:79], a[204:207]\n"
      "ds_read_b64_tr_b16 v[88:89], v117 offset:50688\n"
      "v_mfma_f32_16x16x32_bf16 a[140:143], v[48:51], v[76:79], a[140:143]\n"
      "v_add_f32 v112, v112, v4\n"
      "v_mfma_f32_16x16x32_bf16 a[136:139], v[48:51], v[72:75], a[136:139]\n"
      "s_add_u32 s36, s36, 16384\n"
      "ds_read_b64_tr_b16 v[90:91], v117 offset:58880\n"
      "v_mfma_f32_16x16x32_bf16 v[200:203], v[40:43], v[72:75], v[200:203]\n"
      "v_exp_f32 v5, v5\n"
      "v_mfma_f32_16x16x32_bf16 v[204:207], v[40:43], v[76:79], v[204:207]\n"
      "ds_read_b64_tr_b16 v[92:93], v118 offset:50688\n"
      "v_mfma_f32_16x16x32_bf16 v[140:143], v[32:35], v[76:79], v[140:143]\n"
      "v_add_f32 v112, v112, v5\n"
      "v_mfma_f32_16x16x32_bf16 v[136:139], v[32:35], v[72:75], v[136:139]\n"
      "s_waitcnt lgkmcnt(3)\n"
      "ds_read_b64_tr_b16 v[94:95], v118 offset:58880\n"
      "v_cvt_pk_bf16_f32 v2, v4, v5\n"
      "v_mfma_f32_16x16x32_bf16 v[144:147], v[32:35], v[80:83], v[144:147]\n"
      "v_exp_f32 v6, v6\n"
      "v_mfma_f32_16x16x32_bf16 v[148:151], v[32:35], v[84:87], v[148:151]\n"
      "ds_read_b64_tr_b16 v[64:65], v117 offset:51200\n"
      "v_mfma_f32_16x16x32_bf16 v[212:215], v[40:43], v[84:87], v[212:215]\n"
      "ds_read_b64_tr_b16 v[66:67], v117 offset:59392\n"
      "v_add_f32 v112, v112, v6\n"
      "v_mfma_f32_16x16x32_bf16 v[208:211], v[40:43], v[80:83], v[208:211]\n"
      "ds_read_b64_tr_b16 v[68:69], v118 offset:51200\n"
      "v_mfma_f32_16x16x32_bf16 a[144:147], v[48:51], v[80:83], a[144:147]\n"
      "v_exp_f32 v7, v7\n"
      "v_mfma_f32_16x16x32_bf16 a[148:151], v[48:51], v[84:87], a[148:151]\n"
      "ds_read_b64_tr_b16 v[70:71], v118 offset:59392\n"
      "v_mfma_f32_16x16x32_bf16 a[212:215], v[56:59], v[84:87], a[212:215]\n"
      "s_add_u32 m0, s92, 99200\n"
      "v_add_f32 v112, v112, v7\n"
      "v_cvt_pk_bf16_f32 v3, v6, v7\n"
      "v_mfma_f32_16x16x32_bf16 a[208:211], v[56:59], v[80:83], a[208:211]\n"
      "s_waitcnt lgkmcnt(4)\n"
      "buffer_load_dwordx4 v120, s[24:27], s37 offen offset:128 nt lds\n"
      "v_mfma_f32_16x16x32_bf16 a[216:219], v[56:59], v[88:91], a[216:219]\n"
      "v_exp_f32 v8, v8\n"
      "v_mfma_f32_16x16x32_bf16 a[220:223], v[56:59], v[92:95], a[220:223]\n"
      "ds_read_b64_tr_b16 v[72:73], v117 offset:51712\n"
      "v_mfma_f32_16x16x32_bf16 a[156:159], v[48:51], v[92:95], a[156:159]\n"
      "v_add_f32 v113, v113, v8\n"
      "v_mfma_f32_16x16x32_bf16 a[152:155], v[48:51], v[88:91], a[152:155]\n"
      "ds_read_b64_tr_b16 v[74:75], v117 offset:59904\n"
      "v_mfma_f32_16x16x32_bf16 v[216:219], v[40:43], v[88:91], v[216:219]\n"
      "v_exp_f32 v9, v9\n"
      "v_mfma_f32_16x16x32_bf16 v[220:223], v[40:43], v[92:95], v[220:223]\n"
      "ds_read_b64_tr_b16 v[76:77], v118 offset:51712\n"
      "v_mfma_f32_16x16x32_bf16 v[156:159], v[32:35], v[92:95], v[156:159]\n"
      "v_add_f32 v113, v113, v9\n"
      "v_mfma_f32_16x16x32_bf16 v[152:155], v[32:35], v[88:91], v[152:155]\n"
      "s_waitcnt lgkmcnt(3)\n"
      "ds_read_b64_tr_b16 v[78:79], v118 offset:59904\n"
      "v_cvt_pk_bf16_f32 v8, v8, v9\n"
      "v_mfma_f32_16x16x32_bf16 v[160:163], v[32:35], v[64:67], v[160:163]\n"
      "v_exp_f32 v10, v10\n"
      "v_mfma_f32_16x16x32_bf16 v[164:167], v[32:35], v[68:71], v[164:167]\n"
      "ds_read_b64_tr_b16 v[80:81], v117 offset:52224\n"
      "v_mfma_f32_16x16x32_bf16 v[228:231], v[40:43], v[68:71], v[228:231]\n"
      "ds_read_b64_tr_b16 v[82:83], v117 offset:60416\n"
      "v_add_f32 v113, v113, v10\n"
      "v_mfma_f32_16x16x32_bf16 v[224:227], v[40:43], v[64:67], v[224:227]\n"
      "ds_read_b64_tr_b16 v[84:85], v118 offset:52224\n"
      "v_mfma_f32_16x16x32_bf16 a[160:163], v[48:51], v[64:67], a[160:163]\n"
      "v_exp_f32 v11, v11\n"
      "v_mfma_f32_16x16x32_bf16 a[164:167], v[48:51], v[68:71], a[164:167]\n"
      "ds_read_b64_tr_b16 v[86:87], v118 offset:60416\n"
      "v_mfma_f32_16x16x32_bf16 a[228:231], v[56:59], v[68:71], a[228:231]\n"
      "s_add_u32 m0, s92, 100096\n"
      "v_add_f32 v113, v113, v11\n"
      "v_cvt_pk_bf16_f32 v9, v10, v11\n"
      "v_mfma_f32_16x16x32_bf16 a[224:227], v[56:59], v[64:67], a[224:227]\n"
      "s_waitcnt lgkmcnt(4)\n"
      "buffer_load_dwordx4 v120, s[24:27], s37 offen offset:256 nt lds\n"
      "v_mfma_f32_16x16x32_bf16 a[232:235], v[56:59], v[72:75], a[232:235]\n"
      "v_exp_f32 v12, v12\n"
      "v_mfma_f32_16x16x32_bf16 a[236:239], v[56:59], v[76:79], a[236:239]\n"
      "ds_read_b64_tr_b16 v[88:89], v117 offset:52736\n"
      "v_mfma_f32_16x16x32_bf16 a[172:175], v[48:51], v[76:79], a[172:175]\n"
      "v_add_f32 v113, v113, v12\n"
      "v_mfma_f32_16x16x32_bf16 a[168:171], v[48:51], v[72:75], a[168:171]\n"
      "ds_read_b64_tr_b16 v[90:91], v117 offset:60928\n"
      "v_mfma_f32_16x16x32_bf16 v[232:235], v[40:43], v[72:75], v[232:235]\n"
      "v_exp_f32 v13, v13\n"
      "v_mfma_f32_16x16x32_bf16 v[236:239], v[40:43], v[76:79], v[236:239]\n"
      "ds_read_b64_tr_b16 v[92:93], v118 offset:52736\n"
      "v_mfma_f32_16x16x32_bf16 v[172:175], v[32:35], v[76:79], v[172:175]\n"
      "v_add_f32 v113, v113, v13\n"
      "v_mfma_f32_16x16x32_bf16 v[168:171], v[32:35], v[72:75], v[168:171]\n"
      "s_waitcnt lgkmcnt(3)\n"
      "ds_read_b64_tr_b16 v[94:95], v118 offset:60928\n"
      "v_cvt_pk_bf16_f32 v10, v12, v13\n"
      "v_mfma_f32_16x16x32_bf16 v[176:179], v[32:35], v[80:83], v[176:179]\n"
      "v_exp_f32 v14, v14\n"
      "v_mfma_f32_16x16x32_bf16 v[180:183], v[32:35], v[84:87], v[180:183]\n"
      "ds_read_b128 v[64:67], v116 offset:16384\n"
      "v_mfma_f32_16x16x32_bf16 v[244:247], v[40:43], v[84:87], v[244:247]\n"
      "v_add_f32 v113, v113, v14\n"
      "v_mfma_f32_16x16x32_bf16 v[240:243], v[40:43], v[80:83], v[240:243]\n"
      "ds_read_b128 v[68:71], v116 offset:16896\n"
      "v_mfma_f32_16x16x32_bf16 a[176:179], v[48:51], v[80:83], a[176:179]\n"
      "v_exp_f32 v15, v15\n"
      "v_mfma_f32_16x16x32_bf16 a[180:183], v[48:51], v[84:87], a[180:183]\n"
      "v_mfma_f32_16x16x32_bf16 a[244:247], v[56:59], v[84:87], a[244:247]\n"
      "s_add_u32 m0, s92, 100992\n"
      "v_add_f32 v113, v113, v15\n"
      "v_cvt_pk_bf16_f32 v11, v14, v15\n"
      "v_mfma_f32_16x16x32_bf16 a[240:243], v[56:59], v[80:83], a[240:243]\n"
      "s_waitcnt lgkmcnt(2)\n"
      "buffer_load_dwordx4 v120, s[24:27], s37 offen offset:384 nt lds\n"
      "v_mfma_f32_16x16x32_bf16 a[248:251], v[56:59], v[88:91], a[248:251]\n"
      "v_exp_f32 v16, v16\n"
      "v_mfma_f32_16x16x32_bf16 a[252:255], v[56:59], v[92:95], a[252:255]\n"
      "s_add_u32 s37, s37, 16384\n"
      "ds_read_b128 v[72:75], v116 offset:17408\n"
      "v_mfma_f32_16x16x32_bf16 a[188:191], v[48:51], v[92:95], a[188:191]\n"
      "v_add_f32 v114, v114, v16\n"
      "v_mfma_f32_16x16x32_bf16 a[184:187], v[48:51], v[88:91], a[184:187]\n"
      "v_mfma_f32_16x16x32_bf16 v[248:251], v[40:43], v[88:91], v[248:251]\n"
      "v_exp_f32 v17, v17\n"
      "v_mfma_f32_16x16x32_bf16 v[252:255], v[40:43], v[92:95], v[252:255]\n"
      "ds_read_b128 v[76:79], v116 offset:17920\n"
      "v_mfma_f32_16x16x32_bf16 v[188:191], v[32:35], v[92:95], v[188:191]\n"
      "v_add_f32 v114, v114, v17\n"
      "v_mfma_f32_16x16x32_bf16 v[184:187], v[32:35], v[88:91], v[184:187]\n"
      "s_waitcnt lgkmcnt(2)\n"
      "v_cvt_pk_bf16_f32 v16, v16, v17\n"
      "v_mfma_f32_16x16x32_bf16 v[32:35], v[64:67], a[0:3], v[96:99]\n"
      "v_exp_f32 v18, v18\n"
      "v_mfma_f32_16x16x32_bf16 v[32:35], v[68:71], a[4:7], v[32:35]\n"
      "s_waitcnt vmcnt(12)\n"
      "s_barrier\n"
      "ds_read_b128 v[80:83], v116 offset:18432\n"
      "v_mfma_f32_16x16x32_bf16 v[40:43], v[68:71], a[36:39], v[100:103]\n"
      "v_add_f32 v114, v114, v18\n"
      "v_mfma_f32_16x16x32_bf16 v[40:43], v[64:67], a[32:35], v[40:43]\n"
      "ds_read_b128 v[84:87], v116 offset:18944\n"
      "v_mfma_f32_16x16x32_bf16 v[48:51], v[64:67], a[64:67], v[104:107]\n"
      "v_exp_f32 v19, v19\n"
      "v_mfma_f32_16x16x32_bf16 v[48:51], v[68:71], a[68:71], v[48:51]\n"
      "v_mfma_f32_16x16x32_bf16 v[56:59], v[68:71], a[100:103], v[108:111]\n"
      "s_add_u32 m0, s92, 0\n"
      "v_add_f32 v114, v114, v19\n"
      "v_cvt_pk_bf16_f32 v17, v18, v19\n"
      "v_mfma_f32_16x16x32_bf16 v[56:59], v[64:67], a[96:99], v[56:59]\n"
      "s_waitcnt lgkmcnt(2)\n"
      "buffer_load_dwordx4 v119, s[20:23], s36 offen offset:0 nt lds\n"
      "v_mfma_f32_16x16x32_bf16 v[56:59], v[72:75], a[104:107], v[56:59]\n"
      "v_exp_f32 v20, v20\n"
      "v_mfma_f32_16x16x32_bf16 v[56:59], v[76:79], a[108:111], v[56:59]\n"
      "ds_read_b128 v[88:91], v116 offset:19456\n"
      "v_mfma_f32_16x16x32_bf16 v[48:51], v[76:79], a[76:79], v[48:51]\n"
      "v_add_f32 v114, v114, v20\n"
      "v_mfma_f32_16x16x32_bf16 v[48:51], v[72:75], a[72:75], v[48:51]\n"
      "s_cmp_eq_u32 s39, 1\n"
      "s_cbranch_scc1 LATE5_%=\n"
      "BACK5_%=:\n"
      "v_mfma_f32_16x16x32_bf16 v[40:43], v[72:75], a[40:43], v[40:43]\n"
      "v_exp_f32 v21, v21\n"
      "v_mfma_f32_16x16x32_bf16 v[40:43], v[76:79], a[44:47], v[40:43]\n"
      "ds_read_b128 v[92:95], v116 offset:19968\n"
      "v_mfma_f32_16x16x32_bf16 v[32:35], v[76:79], a[12:15], v[32:35]\n"
      "v_add_f32 v114, v114, v21\n"
      "v_mfma_f32_16x16x32_bf16 v[32:35], v[72:75], a[8:11], v[32:35]\n"
      "s_waitcnt lgkmcnt(2)\n"
      "v_cvt_pk_bf16_f32 v18, v20, v21\n"
      "v_mfma_f32_16x16x32_bf16 v[32:35], v[80:83], a[16:19], v[32:35]\n"
      "v_exp_f32 v22, v22\n"
      "v_mfma_f32_16x16x32_bf16 v[32:35], v[84:87], a[20:23], v[32:35]\n"
      "ds_read_b128 v[64:67], v116 offset:24576\n"
      "v_mfma_f32_16x16x32_bf16 v[40:43], v[84:87], a[52:55], v[40:43]\n"
      "v_add_f32 v114, v114, v22\n"
      "v_mfma_f32_16x16x32_bf16 v[40:43], v[80:83], a[48:51], v[40:43]\n"
      "ds_read_b128 v[68:71], v116 offset:25088\n"
      "v_mfma_f32_16x16x32_bf16 v[48:51], v[80:83], a[80:83], v[48:51]\n"
      "v_exp_f32 v23, v23\n"
      "v_mfma_f32_16x16x32_bf16 v[48:51], v[84:87], a[84:87], v[48:51]\n"
      "v_mfma_f32_16x16x32_bf16 v[56:59], v[84:87], a[116:119], v[56:59]\n"
      "s_add_u32 m0, s92, 896\n"
      "v_add_f32 v114, v114, v23\n"
      "v_cvt_pk_bf16_f32 v19, v22, v23\n"
      "v_mfma_f32_16x16x32_bf16 v[56:59], v[80:83], a[112:115], v[56:59]\n"
      "s_waitcnt lgkmcnt(2)\n"
      "buffer_load_dwordx4 v119, s[20:23], s36 offen offset:128 nt lds\n"
      "v_mfma_f32_16x16x32_bf16 v[56:59], v[88:91], a[120:123], v[56:59]\n"
      "v_exp_f32 v24, v24\n"
      "v_mfma_f32_16x16x32_bf16 v[56:59], v[92:95], a[124:127], v[56:59]\n"
      "ds_read_b128 v[72:75], v116 offset:25600\n"
      "v_mfma_f32_16x16x32_bf16 v[48:51], v[92:95], a[92:95], v[48:51]\n"
      "v_add_f32 v115, v115, v24\n"
      "v_mfma_f32_16x16x32_bf16 v[48:51], v[88:91], a[88:91], v[48:51]\n"
      "v_mfma_f32_16x16x32_bf16 v[40:43], v[88:91], a[56:59], v[40:43]\n"
      "v_exp_f32 v25, v25\n"
      "v_mfma_f32_16x16x32_bf16 v[40:43], v[92:95], a[60:63], v[40:43]\n"
      "ds_read_b128 v[76:79], v116 offset:26112\n"
      "v_mfma_f32_16x16x32_bf16 v[32:35], v[92:95], a[28:31], v[32:35]\n"
      "v_add_f32 v115, v115, v25\n"
      "v_mfma_f32_16x16x32_bf16 v[32:35], v[88:91], a[24:27], v[32:35]\n"
      "s_waitcnt lgkmcnt(2)\n"
      "v_cvt_pk_bf16_f32 v24, v24, v25\n"
      "v_mfma_f32_16x16x32_bf16 v[36:39], v[64:67], a[0:3], v[96:99]\n"
      "v_exp_f32 v26, v26\n"
      "v_mfma_f32_16x16x32_bf16 v[36:39], v[68:71], a[4:7], v[36:39]\n"
      "ds_read_b128 v[80:83], v116 offset:26624\n"
      "v_mfma_f32_16x16x32_bf16 v[44:47], v[68:71], a[36:39], v[100:103]\n"
      "v_add_f32 v115, v115, v26\n"
      "v_mfma_f32_16x16x32_bf16 v[44:47], v[64:67], a[32:35], v[44:47]\n"
      "ds_read_b128 v[84:87], v116 offset:27136\n"
      "v_mfma_f32_16x16x32_bf16 v[52:55], v[64:67], a[64:67], v[104:107]\n"
      "v_exp_f32 v27, v27\n"
      "v_mfma_f32_16x16x32_bf16 v[52:55], v[68:71], a[68:71], v[52:55]\n"
      "v_mfma_f32_16x16x32_bf16 v[60:63], v[68:71], a[100:103], v[108:111]\n"
      "s_add_u32 m0, s92, 1792\n"
      "v_add_f32 v115, v115, v27\n"
      "v_cvt_pk_bf16_f32 v25, v26, v27\n"
      "v_mfma_f32_16x16x32_bf16 v[60:63], v[64:67], a[96:99], v[60:63]\n"
      "s_waitcnt lgkmcnt(2)\n"
      "buffer_load_dwordx4 v119, s[20:23], s36 offen offset:256 nt lds\n"
      "v_mfma_f32_16x16x32_bf16 v[60:63], v[72:75], a[104:107], v[60:63]\n"
      "v_exp_f32 v28, v28\n"
      "v_mfma_f32_16x16x32_bf16 v[60:63], v[76:79], a[108:111], v[60:63]\n"
      "ds_read_b128 v[88:91], v116 offset:27648\n"
      "v_mfma_f32_16x16x32_bf16 v[52:55], v[76:79], a[76:79], v[52:55]\n"
      "v_add_f32 v115, v115, v28\n"
      "v_mfma_f32_16x16x32_bf16 v[52:55], v[72:75], a[72:75], v[52:55]\n"
      "v_mfma_f32_16x16x32_bf16 v[44:47], v[72:75], a[40:43], v[44:47]\n"
      "v_exp_f32 v29, v29\n"
      "v_mfma_f32_16x16x32_bf16 v[44:47], v[76:79], a[44:47], v[44:47]\n"
      "ds_read_b128 v[92:95], v116 offset:28160\n"
      "v_mfma_f32_16x16x32_bf16 v[36:39], v[76:79], a[12:15], v[36:39]\n"
      "v_add_f32 v115, v115, v29\n"
      "v_mfma_f32_16x16x32_bf16 v[36:39], v[72:75], a[8:11], v[36:39]\n"
      "s_waitcnt lgkmcnt(2)\n"
      "v_cvt_pk_bf16_f32 v26, v28, v29\n"
      "v_mfma_f32_16x16x32_bf16 v[36:39], v[80:83], a[16:19], v[36:39]\n"
      "v_exp_f32 v30, v30\n"
      "v_mfma_f32_16x16x32_bf16 v[36:39], v[84:87], a[20:23], v[36:39]\n"
      "ds_read_b64_tr_b16 v[64:65], v117 offset:0\n"
      "v_mfma_f32_16x16x32_bf16 v[44:47], v[84:87], a[52:55], v[44:47]\n"
      "ds_read_b64_tr_b16 v[66:67], v117 offset:8192\n"
      "v_add_f32 v115, v115, v30\n"
      "v_mfma_f32_16x16x32_bf16 v[44:47], v[80:83], a[48:51], v[44:47]\n"
      "ds_read_b64_tr_b16 v[68:69], v118 offset:0\n"
      "v_mfma_f32_16x16x32_bf16 v[52:55], v[80:83], a[80:83], v[52:55]\n"
      "v_exp_f32 v31, v31\n"
      "v_mfma_f32_16x16x32_bf16 v[52:55], v[84:87], a[84:87], v[52:55]\n"
      "ds_read_b64_tr_b16 v[70:71], v118 offset:8192\n"
      "v_mfma_f32_16x16x32_bf16 v[60:63], v[84:87], a[116:119], v[60:63]\n"
      "s_add_u32 m0, s92, 2688\n"
      "v_add_f32 v115, v115, v31\n"
      "v_cvt_pk_bf16_f32 v27, v30, v31\n"
      "v_mfma_f32_16x16x32_bf16 v[60:63], v[80:83], a[112:115], v[60:63]\n"
      "s_waitcnt lgkmcnt(4)\n"
      "buffer_load_dwordx4 v119, s[20:23], s36 offen offset:384 nt lds\n"
      "v_mfma_f32_16x16x32_bf16 v[60:63], v[88:91], a[120:123], v[60:63]\n"
      "v_exp_f32 v32, v32\n"
      "v_mfma_f32_16x16x32_bf16 v[60:63], v[92:95], a[124:127], v[60:63]\n"
      "ds_read_b64_tr_b16 v[72:73], v117 offset:512\n"
      "v_mfma_f32_16x16x32_bf16 v[52:55], v[92:95], a[92:95], v[52:55]\n"
      "v_add_f32 v112, v112, v32\n"
      "v_mfma_f32_16x16x32_bf16 v[52:55], v[88:91], a[88:91], v[52:55]\n"
      "ds_read_b64_tr_b16 v[74:75], v117 offset:8704\n"
      "v_mfma_f32_16x16x32_bf16 v[44:47], v[88:91], a[56:59], v[44:47]\n"
      "v_exp_f32 v33, v33\n"
      "v_mfma_f32_16x16x32_bf16 v[44:47], v[92:95], a[60:63], v[44:47]\n"
      "ds_read_b64_tr_b16 v[76:77], v118 offset:512\n"
      "v_mfma_f32_16x16x32_bf16 v[36:39], v[92:95], a[28:31], v[36:39]\n"
      "v_add_f32 v112, v112, v33\n"
      "v_mfma_f32_16x16x32_bf16 v[36:39], v[88:91], a[24:27], v[36:39]\n"
      "s_waitcnt lgkmcnt(3)\n"
      "ds_read_b64_tr_b16 v[78:79], v118 offset:8704\n"
      "v_cvt_pk_bf16_f32 v32, v32, v33\n"
      "v_mfma_f32_16x16x32_bf16 v[128:131], v[0:3], v[64:67], v[128:131]\n"
      "v_exp_f32 v34, v34\n"
      "v_mfma_f32_16x16x32_bf16 v[132:135], v[0:3], v[68:71], v[132:135]\n"
      "ds_read_b64_tr_b16 v[80:81], v117 offset:1024\n"
      "v_mfma_f32_16x16x32_bf16 v[196:199], v[8:11], v[68:71], v[196:199]\n"
      "ds_read_b64_tr_b16 v[82:83], v117 offset:9216\n"
      "v_add_f32 v112, v112, v34\n"
      "v_mfma_f32_16x16x32_bf16 v[192:195], v[8:11], v[64:67], v[192:195]\n"
      "ds_read_b64_tr_b16 v[84:85], v118 offset:1024\n"
      "v_mfma_f32_16x16x32_bf16 a[128:131], v[16:19], v[64:67], a[128:131]\n"
      "v_exp_f32 v35, v35\n"
      "v_mfma_f32_16x16x32_bf16 a[132:135], v[16:19], v[68:71], a[132:135]\n"
      "ds_read_b64_tr_b16 v[86:87], v118 offset:9216\n"
      "v_mfma_f32_16x16x32_bf16 a[196:199], v[24:27], v[68:71], a[196:199]\n"
      "s_add_u32 m0, s92, 114688\n"
      "v_add_f32 v112, v112, v35\n"
      "v_cvt_pk_bf16_f32 v33, v34, v35\n"
      "v_mfma_f32_16x16x32_bf16 a[192:195], v[24:27], v[64:67], a[192:195]\n"
      "s_waitcnt lgkmcnt(4)\n"
      "buffer_load_dwordx4 v120, s[24:27], s37 offen offset:0 nt lds\n"
      "v_mfma_f32_16x16x32_bf16 a[200:203], v[24:27], v[72:75], a[200:203]\n"
      "v_exp_f32 v36, v36\n"
      "v_mfma_f32_16x16x32_bf16 a[204:207], v[24:27], v[76:79], a[204:207]\n"
      "ds_read_b64_tr_b16 v[88:89], v117 offset:1536\n"
      "v_mfma_f32_16x16x32_bf16 a[140:143], v[16:19], v[76:79], a[140:143]\n"
      "v_add_f32 v112, v112, v36\n"
      "v_mfma_f32_16x16x32_bf16 a[136:139], v[16:19], v[72:75], a[136:139]\n"
      "s_add_u32 s36, s36, 16384\n"
      "ds_read_b64_tr_b16 v[90:91], v117 offset:9728\n"
      "v_mfma_f32_16x16x32_bf16 v[200:203], v[8:11], v[72:75], v[200:203]\n"
      "v_exp_f32 v37, v37\n"
      "v_mfma_f32_16x16x32_bf16 v[204:207], v[8:11], v[76:79], v[204:207]\n"
      "ds_read_b64_tr_b16 v[92:93], v118 offset:1536\n"
      "v_mfma_f32_16x16x32_bf16 v[140:143], v[0:3], v[76:79], v[140:143]\n"
      "v_add_f32 v112, v112, v37\n"
      "v_mfma_f32_16x16x32_bf16 v[136:139], v[0:3], v[72:75], v[136:139]\n"
      "s_waitcnt lgkmcnt(3)\n"
      "ds_read_b64_tr_b16 v[94:95], v118 offset:9728\n"
      "v_cvt_pk_bf16_f32 v34, v36, v37\n"
      "v_mfma_f32_16x16x32_bf16 v[144:147], v[0:3], v[80:83], v[144:147]\n"
      "v_exp_f32 v38, v38\n"
      "v_mfma_f32_16x16x32_bf16 v[148:151], v[0:3], v[84:87], v[148:151]\n"
      "ds_read_b64_tr_b16 v[64:65], v117 offset:2048\n"
      "v_mfma_f32_16x16x32_bf16 v[212:215], v[8:11], v[84:87], v[212:215]\n"
      "ds_read_b64_tr_b16 v[66:67], v117 offset:10240\n"
      "v_add_f32 v112, v112, v38\n"
      "v_mfma_f32_16x16x32_bf16 v[208:211], v[8:11], v[80:83], v[208:211]\n"
      "ds_read_b64_tr_b16 v[68:69], v118 offset:2048\n"
      "v_mfma_f32_16x16x32_bf16 a[144:147], v[16:19], v[80:83], a[144:147]\n"
      "v_exp_f32 v39, v39\n"
      "v_mfma_f32_16x16x32_bf16 a[148:151], v[16:19], v[84:87], a[148:151]\n"
      "ds_read_b64_tr_b16 v[70:71], v118 offset:10240\n"
      "v_mfma_f32_16x16x32_bf16 a[212:215], v[24:27], v[84:87], a[212:215]\n"
      "s_add_u32 m0, s92, 115584\n"
      "v_add_f32 v112, v112, v39\n"
      "v_cvt_pk_bf16_f32 v35, v38, v39\n"
      "v_mfma_f32_16x16x32_bf16 a[208:211], v[24:27], v[80:83], a[208:211]\n"
      "s_waitcnt lgkmcnt(4)\n"
      "buffer_load_dwordx4 v120, s[24:27], s37 offen offset:128 nt lds\n"
      "v_mfma_f32_16x16x32_bf16 a[216:219], v[24:27], v[88:91], a[216:219]\n"
      "v_exp_f32 v40, v40\n"
      "v_mfma_f32_16x16x32_bf16 a[220:223], v[24:27], v[92:95], a[220:223]\n"
      "ds_read_b64_tr_b16 v[72:73], v117 offset:2560\n"
      "v_mfma_f32_16x16x32_bf16 a[156:159], v[16:19], v[92:95], a[156:159]\n"
      "v_add_f32 v113, v113, v40\n"
      "v_mfma_f32_16x16x32_bf16 a[152:155], v[16:19], v[88:91], a[152:155]\n"
      "ds_read_b64_tr_b16 v[74:75], v117 offset:10752\n"
      "v_mfma_f32_16x16x32_bf16 v[216:219], v[8:11], v[88:91], v[216:219]\n"
      "v_exp_f32 v41, v41\n"
      "v_mfma_f32_16x16x32_bf16 v[220:223], v[8:11], v[92:95], v[220:223]\n"
      "ds_read_b64_tr_b16 v[76:77], v118 offset:2560\n"
      "v_mfma_f32_16x16x32_bf16 v[156:159], v[0:3], v[92:95], v[156:159]\n"
      "v_add_f32 v113, v113, v41\n"
      "v_mfma_f32_16x16x32_bf16 v[152:155], v[0:3], v[88:91], v[152:155]\n"
      "s_waitcnt lgkmcnt(3)\n"
      "ds_read_b64_tr_b16 v[78:79], v118 offset:10752\n"
      "v_cvt_pk_bf16_f32 v40, v40, v41\n"
      "v_mfma_f32_16x16x32_bf16 v[160:163], v[0:3], v[64:67], v[160:163]\n"
      "v_exp_f32 v42, v42\n"
      "v_mfma_f32_16x16x32_bf16 v[164:167], v[0:3], v[68:71], v[164:167]\n"
      "ds_read_b64_tr_b16 v[80:81], v117 offset:3072\n"
      "v_mfma_f32_16x16x32_bf16 v[228:231], v[8:11], v[68:71], v[228:231]\n"
      "ds_read_b64_tr_b16 v[82:83], v117 offset:11264\n"
      "v_add_f32 v113, v113, v42\n"
      "v_mfma_f32_16x16x32_bf16 v[224:227], v[8:11], v[64:67], v[224:227]\n"
      "ds_read_b64_tr_b16 v[84:85], v118 offset:3072\n"
      "v_mfma_f32_16x16x32_bf16 a[160:163], v[16:19], v[64:67], a[160:163]\n"
      "v_exp_f32 v43, v43\n"
      "v_mfma_f32_16x16x32_bf16 a[164:167], v[16:19], v[68:71], a[164:167]\n"
      "ds_read_b64_tr_b16 v[86:87], v118 offset:11264\n"
      "v_mfma_f32_16x16x32_bf16 a[228:231], v[24:27], v[68:71], a[228:231]\n"
      "s_add_u32 m0, s92, 116480\n"
      "v_add_f32 v113, v113, v43\n"
      "v_cvt_pk_bf16_f32 v41, v42, v43\n"
      "v_mfma_f32_16x16x32_bf16 a[224:227], v[24:27], v[64:67], a[224:227]\n"
      "s_waitcnt lgkmcnt(4)\n"
      "buffer_load_dwordx4 v120, s[24:27], s37 offen offset:256 nt lds\n"
      "v_mfma_f32_16x16x32_bf16 a[232:235], v[24:27], v[72:75], a[232:235]\n"
      "v_exp_f32 v44, v44\n"
      "v_mfma_f32_16x16x32_bf16 a[236:239], v[24:27], v[76:79], a[236:239]\n"
      "ds_read_b64_tr_b16 v[88:89], v117 offset:3584\n"
      "v_mfma_f32_16x16x32_bf16 a[172:175], v[16:19], v[76:79], a[172:175]\n"
      "v_add_f32 v113, v113, v44\n"
      "v_mfma_f32_16x16x32_bf16 a[168:171], v[16:19], v[72:75], a[168:171]\n"
      "ds_read_b64_tr_b16 v[90:91], v117 offset:11776\n"
      "v_mfma_f32_16x16x32_bf16 v[232:235], v[8:11], v[72:75], v[232:235]\n"
      "v_exp_f32 v45, v45\n"
      "v_mfma_f32_16x16x32_bf16 v[236:239], v[8:11], v[76:79], v[236:239]\n"
      "ds_read_b64_tr_b16 v[92:93], v118 offset:3584\n"
      "v_mfma_f32_16x16x32_bf16 v[172:175], v[0:3], v[76:79], v[172:175]\n"
      "v_add_f32 v113, v113, v45\n"
      "v_mfma_f32_16x16x32_bf16 v[168:171], v[0:3], v[72:75], v[168:171]\n"
      "s_waitcnt lgkmcnt(3)\n"
      "ds_read_b64_tr_b16 v[94:95], v118 offset:11776\n"
      "v_cvt_pk_bf16_f32 v42, v44, v45\n"
      "v_mfma_f32_16x16x32_bf16 v[176:179], v[0:3], v[80:83], v[176:179]\n"
      "v_exp_f32 v46, v46\n"
      "v_mfma_f32_16x16x32_bf16 v[180:183], v[0:3], v[84:87], v[180:183]\n"
      "ds_read_b128 v[64:67], v116 offset:32768\n"
      "v_mfma_f32_16x16x32_bf16 v[244:247], v[8:11], v[84:87], v[244:247]\n"
      "v_add_f32 v113, v113, v46\n"
      "v_mfma_f32_16x16x32_bf16 v[240:243], v[8:11], v[80:83], v[240:243]\n"
      "ds_read_b128 v[68:71], v116 offset:33280\n"
      "v_mfma_f32_16x16x32_bf16 a[176:179], v[16:19], v[80:83], a[176:179]\n"
      "v_exp_f32 v47, v47\n"
      "v_mfma_f32_16x16x32_bf16 a[180:183], v[16:19], v[84:87], a[180:183]\n"
      "v_mfma_f32_16x16x32_bf16 a[244:247], v[24:27], v[84:87], a[244:247]\n"
      "s_add_u32 m0, s92, 117376\n"
      "v_add_f32 v113, v113, v47\n"
      "v_cvt_pk_bf16_f32 v43, v46, v47\n"
      "v_mfma_f32_16x16x32_bf16 a[240:243], v[24:27], v[80:83], a[240:243]\n"
      "s_waitcnt lgkmcnt(2)\n"
      "buffer_load_dwordx4 v120, s[24:27], s37 offen offset:384 nt lds\n"
      "v_mfma_f32_16x16x32_bf16 a[248:251], v[24:27], v[88:91], a[248:251]\n"
      "v_exp_f32 v48, v48\n"
      "v_mfma_f32_16x16x32_bf16 a[252:255], v[24:27], v[92:95], a[252:255]\n"
      "s_add_u32 s37, s37, 16384\n"
      "ds_read_b128 v[72:75], v116 offset:33792\n"
      "v_mfma_f32_16x16x32_bf16 a[188:191], v[16:19], v[92:95], a[188:191]\n"
      "v_add_f32 v114, v114, v48\n"
      "v_mfma_f32_16x16x32_bf16 a[184:187], v[16:19], v[88:91], a[184:187]\n"
      "v_mfma_f32_16x16x32_bf16 v[248:251], v[8:11], v[88:91], v[248:251]\n"
      "v_exp_f32 v49, v49\n"
      "v_mfma_f32_16x16x32_bf16 v[252:255], v[8:11], v[92:95], v[252:255]\n"
      "ds_read_b128 v[76:79], v116 offset:34304\n"
      "v_mfma_f32_16x16x32_bf16 v[188:191], v[0:3], v[92:95], v[188:191]\n"
      "v_add_f32 v114, v114, v49\n"
      "v_mfma_f32_16x16x32_bf16 v[184:187], v[0:3], v[88:91], v[184:187]\n"
      "s_waitcnt lgkmcnt(2)\n"
      "v_cvt_pk_bf16_f32 v48, v48, v49\n"
      "v_mfma_f32_16x16x32_bf16 v[0:3], v[64:67], a[0:3], v[96:99]\n"
      "v_exp_f32 v50, v50\n"
      "v_mfma_f32_16x16x32_bf16 v[0:3], v[68:71], a[4:7], v[0:3]\n"
      "s_waitcnt vmcnt(12)\n"
      "s_barrier\n"
      "ds_read_b128 v[80:83], v116 offset:34816\n"
      "v_mfma_f32_16x16x32_bf16 v[8:11], v[68:71], a[36:39], v[100:103]\n"
      "v_add_f32 v114, v114, v50\n"
      "v_mfma_f32_16x16x32_bf16 v[8:11], v[64:67], a[32:35], v[8:11]\n"
      "ds_read_b128 v[84:87], v116 offset:35328\n"
      "v_mfma_f32_16x16x32_bf16 v[16:19], v[64:67], a[64:67], v[104:107]\n"
      "v_exp_f32 v51, v51\n"
      "v_mfma_f32_16x16x32_bf16 v[16:19], v[68:71], a[68:71], v[16:19]\n"
      "v_mfma_f32_16x16x32_bf16 v[24:27], v[68:71], a[100:103], v[108:111]\n"
      "s_add_u32 m0, s92, 16384\n"
      "v_add_f32 v114, v114, v51\n"
      "v_cvt_pk_bf16_f32 v49, v50, v51\n"
      "v_mfma_f32_16x16x32_bf16 v[24:27], v[64:67], a[96:99], v[24:27]\n"
      "s_waitcnt lgkmcnt(2)\n"
      "buffer_load_dwordx4 v119, s[20:23], s36 offen offset:0 nt lds\n"
      "v_mfma_f32_16x16x32_bf16 v[24:27], v[72:75], a[104:107], v[24:27]\n"
      "v_exp_f32 v52, v52\n"
      "v_mfma_f32_16x16x32_bf16 v[24:27], v[76:79], a[108:111], v[24:27]\n"
      "ds_read_b128 v[88:91], v116 offset:35840\n"
      "v_mfma_f32_16x16x32_bf16 v[16:19], v[76:79], a[76:79], v[16:19]\n"
      "v_add_f32 v114, v114, v52\n"
      "v_mfma_f32_16x16x32_bf16 v[16:19], v[72:75], a[72:75], v[16:19]\n"
      "v_mfma_f32_16x16x32_bf16 v[8:11], v[72:75], a[40:43], v[8:11]\n"
      "v_exp_f32 v53, v53\n"
      "v_mfma_f32_16x16x32_bf16 v[8:11], v[76:79], a[44:47], v[8:11]\n"
      "ds_read_b128 v[92:95], v116 offset:36352\n"
      "v_mfma_f32_16x16x32_bf16 v[0:3], v[76:79], a[12:15], v[0:3]\n"
      "v_add_f32 v114, v114, v53\n"
      "v_mfma_f32_16x16x32_bf16 v[0:3], v[72:75], a[8:11], v[0:3]\n"
      "s_waitcnt lgkmcnt(2)\n"
      "v_cvt_pk_bf16_f32 v50, v52, v53\n"
      "v_mfma_f32_16x16x32_bf16 v[0:3], v[80:83], a[16:19], v[0:3]\n"
      "v_exp_f32 v54, v54\n"
      "v_mfma_f32_16x16x32_bf16 v[0:3], v[84:87], a[20:23], v[0:3]\n"
      "ds_read_b128 v[64:67], v116 offset:40960\n"
      "v_mfma_f32_16x16x32_bf16 v[8:11], v[84:87], a[52:55], v[8:11]\n"
      "v_add_f32 v114, v114, v54\n"
      "v_mfma_f32_16x16x32_bf16 v[8:11], v[80:83], a[48:51], v[8:11]\n"
      "ds_read_b128 v[68:71], v116 offset:41472\n"
      "v_mfma_f32_16x16x32_bf16 v[16:19], v[80:83], a[80:83], v[16:19]\n"
      "v_exp_f32 v55, v55\n"
      "v_mfma_f32_16x16x32_bf16 v[16:19], v[84:87], a[84:87], v[16:19]\n"
      "v_mfma_f32_16x16x32_bf16 v[24:27], v[84:87], a[116:119], v[24:27]\n"
      "s_add_u32 m0, s92, 17280\n"
      "v_add_f32 v114, v114, v55\n"
      "v_cvt_pk_bf16_f32 v51, v54, v55\n"
      "v_mfma_f32_16x16x32_bf16 v[24:27], v[80:83], a[112:115], v[24:27]\n"
      "s_waitcnt lgkmcnt(2)\n"
      "buffer_load_dwordx4 v119, s[20:23], s36 offen offset:128 nt lds\n"
      "v_mfma_f32_16x16x32_bf16 v[24:27], v[88:91], a[120:123], v[24:27]\n"
      "v_exp_f32 v56, v56\n"
      "v_mfma_f32_16x16x32_bf16 v[24:27], v[92:95], a[124:127], v[24:27]\n"
      "ds_read_b128 v[72:75], v116 offset:41984\n"
      "v_mfma_f32_16x16x32_bf16 v[16:19], v[92:95], a[92:95], v[16:19]\n"
      "v_add_f32 v115, v115, v56\n"
      "v_mfma_f32_16x16x32_bf16 v[16:19], v[88:91], a[88:91], v[16:19]\n"
      "v_mfma_f32_16x16x32_bf16 v[8:11], v[88:91], a[56:59], v[8:11]\n"
      "v_exp_f32 v57, v57\n"
      "v_mfma_f32_16x16x32_bf16 v[8:11], v[92:95], a[60:63], v[8:11]\n"
      "ds_read_b128 v[76:79], v116 offset:42496\n"
      "v_mfma_f32_16x16x32_bf16 v[0:3], v[92:95], a[28:31], v[0:3]\n"
      "v_add_f32 v115, v115, v57\n"
      "v_mfma_f32_16x16x32_bf16 v[0:3], v[88:91], a[24:27], v[0:3]\n"
      "s_waitcnt lgkmcnt(2)\n"
      "v_cvt_pk_bf16_f32 v56, v56, v57\n"
      "v_mfma_f32_16x16x32_bf16 v[4:7], v[64:67], a[0:3], v[96:99]\n"
      "v_exp_f32 v58, v58\n"
      "v_mfma_f32_16x16x32_bf16 v[4:7], v[68:71], a[4:7], v[4:7]\n"
      "ds_read_b128 v[80:83], v116 offset:43008\n"
      "v_mfma_f32_16x16x32_bf16 v[12:15], v[68:71], a[36:39], v[100:103]\n"
      "v_add_f32 v115, v115, v58\n"
      "v_mfma_f32_16x16x32_bf16 v[12:15], v[64:67], a[32:35], v[12:15]\n"
      "ds_read_b128 v[84:87], v116 offset:43520\n"
      "v_mfma_f32_16x16x32_bf16 v[20:23], v[64:67], a[64:67], v[104:107]\n"
      "v_exp_f32 v59, v59\n"
      "v_mfma_f32_16x16x32_bf16 v[20:23], v[68:71], a[68:71], v[20:23]\n"
      "v_mfma_f32_16x16x32_bf16 v[28:31], v[68:71], a[100:103], v[108:111]\n"
      "s_add_u32 m0, s92, 18176\n"
      "v_add_f32 v115, v115, v59\n"
      "v_cvt_pk_bf16_f32 v57, v58, v59\n"
      "v_mfma_f32_16x16x32_bf16 v[28:31], v[64:67], a[96:99], v[28:31]\n"
      "s_waitcnt lgkmcnt(2)\n"
      "buffer_load_dwordx4 v119, s[20:23], s36 offen offset:256 nt lds\n"
      "v_mfma_f32_16x16x32_bf16 v[28:31], v[72:75], a[104:107], v[28:31]\n"
      "v_exp_f32 v60, v60\n"
      "v_mfma_f32_16x16x32_bf16 v[28:31], v[76:79], a[108:111], v[28:31]\n"
      "ds_read_b128 v[88:91], v116 offset:44032\n"
      "v_mfma_f32_16x16x32_bf16 v[20:23], v[76:79], a[76:79], v[20:23]\n"
      "v_add_f32 v115, v115, v60\n"
      "v_mfma_f32_16x16x32_bf16 v[20:23], v[72:75], a[72:75], v[20:23]\n"
      "v_mfma_f32_16x16x32_bf16 v[12:15], v[72:75], a[40:43], v[12:15]\n"
      "v_exp_f32 v61, v61\n"
      "v_mfma_f32_16x16x32_bf16 v[12:15], v[76:79], a[44:47], v[12:15]\n"
      "ds_read_b128 v[92:95], v116 offset:44544\n"
      "v_mfma_f32_16x16x32_bf16 v[4:7], v[76:79], a[12:15], v[4:7]\n"
      "v_add_f32 v115, v115, v61\n"
      "v_mfma_f32_16x16x32_bf16 v[4:7], v[72:75], a[8:11], v[4:7]\n"
      "s_waitcnt lgkmcnt(2)\n"
      "v_cvt_pk_bf16_f32 v58, v60, v61\n"
      "v_mfma_f32_16x16x32_bf16 v[4:7], v[80:83], a[16:19], v[4:7]\n"
      "v_exp_f32 v62, v62\n"
      "v_mfma_f32_16x16x32_bf16 v[4:7], v[84:87], a[20:23], v[4:7]\n"
      "ds_read_b64_tr_b16 v[64:65], v117 offset:16384\n"
      "v_mfma_f32_16x16x32_bf16 v[12:15], v[84:87], a[52:55], v[12:15]\n"
      "ds_read_b64_tr_b16 v[66:67], v117 offset:24576\n"
      "v_add_f32 v115, v115, v62\n"
      "v_mfma_f32_16x16x32_bf16 v[12:15], v[80:83], a[48:51], v[12:15]\n"
      "ds_read_b64_tr_b16 v[68:69], v118 offset:16384\n"
      "v_mfma_f32_16x16x32_bf16 v[20:23], v[80:83], a[80:83], v[20:23]\n"
      "v_exp_f32 v63, v63\n"
      "v_mfma_f32_16x16x32_bf16 v[20:23], v[84:87], a[84:87], v[20:23]\n"
      "ds_read_b64_tr_b16 v[70:71], v118 offset:24576\n"
      "v_mfma_f32_16x16x32_bf16 v[28:31], v[84:87], a[116:119], v[28:31]\n"
      "s_add_u32 m0, s92, 19072\n"
      "v_add_f32 v115, v115, v63\n"
      "v_cvt_pk_bf16_f32 v59, v62, v63\n"
      "v_mfma_f32_16x16x32_bf16 v[28:31], v[80:83], a[112:115], v[28:31]\n"
      "s_waitcnt lgkmcnt(4)\n"
      "buffer_load_dwordx4 v119, s[20:23], s36 offen offset:384 nt lds\n"
      "v_mfma_f32_16x16x32_bf16 v[28:31], v[88:91], a[120:123], v[28:31]\n"
      "v_exp_f32 v0, v0\n"
      "v_mfma_f32_16x16x32_bf16 v[28:31], v[92:95], a[124:127], v[28:31]\n"
      "ds_read_b64_tr_b16 v[72:73], v117 offset:16896\n"
      "v_mfma_f32_16x16x32_bf16 v[20:23], v[92:95], a[92:95], v[20:23]\n"
      "v_add_f32 v112, v112, v0\n"
      "v_mfma_f32_16x16x32_bf16 v[20:23], v[88:91], a[88:91], v[20:23]\n"
      "ds_read_b64_tr_b16 v[74:75], v117 offset:25088\n"
      "v_mfma_f32_16x16x32_bf16 v[12:15], v[88:91], a[56:59], v[12:15]\n"
      "v_exp_f32 v1, v1\n"
      "v_mfma_f32_16x16x32_bf16 v[12:15], v[92:95], a[60:63], v[12:15]\n"
      "ds_read_b64_tr_b16 v[76:77], v118 offset:16896\n"
      "v_mfma_f32_16x16x32_bf16 v[4:7], v[92:95], a[28:31], v[4:7]\n"
      "v_add_f32 v112, v112, v1\n"
      "v_mfma_f32_16x16x32_bf16 v[4:7], v[88:91], a[24:27], v[4:7]\n"
      "s_waitcnt lgkmcnt(3)\n"
      "ds_read_b64_tr_b16 v[78:79], v118 offset:25088\n"
      "v_cvt_pk_bf16_f32 v0, v0, v1\n"
      "v_mfma_f32_16x16x32_bf16 v[128:131], v[32:35], v[64:67], v[128:131]\n"
      "v_exp_f32 v2, v2\n"
      "v_mfma_f32_16x16x32_bf16 v[132:135], v[32:35], v[68:71], v[132:135]\n"
      "ds_read_b64_tr_b16 v[80:81], v117 offset:17408\n"
      "v_mfma_f32_16x16x32_bf16 v[196:199], v[40:43], v[68:71], v[196:199]\n"
      "ds_read_b64_tr_b16 v[82:83], v117 offset:25600\n"
      "v_add_f32 v112, v112, v2\n"
      "v_mfma_f32_16x16x32_bf16 v[192:195], v[40:43], v[64:67], v[192:195]\n"
      "ds_read_b64_tr_b16 v[84:85], v118 offset:17408\n"
      "v_mfma_f32_16x16x32_bf16 a[128:131], v[48:51], v[64:67], a[128:131]\n"
      "v_exp_f32 v3, v3\n"
      "v_mfma_f32_16x16x32_bf16 a[132:135], v[48:51], v[68:71], a[132:135]\n"
      "ds_read_b64_tr_b16 v[86:87], v118 offset:25600\n"
      "v_mfma_f32_16x16x32_bf16 a[196:199], v[56:59], v[68:71], a[196:199]\n"
      "s_add_u32 m0, s92, 65536\n"
      "v_add_f32 v112, v112, v3\n"
      "v_cvt_pk_bf16_f32 v1, v2, v3\n"
      "v_mfma_f32_16x16x32_bf16 a[192:195], v[56:59], v[64:67], a[192:195]\n"
      "s_waitcnt lgkmcnt(4)\n"
      "buffer_load_dwordx4 v120, s[24:27], s37 offen offset:0 nt lds\n"
      "v_mfma_f32_16x16x32_bf16 a[200:203], v[56:59], v[72:75], a[200:203]\n"
      "v_exp_f32 v4, v4\n"
      "v_mfma_f32_16x16x32_bf16 a[204:207], v[56:59], v[76:79], a[204:207]\n"
      "ds_read_b64_tr_b16 v[88:89], v117 offset:17920\n"
      "v_mfma_f32_16x16x32_bf16 a[140:143], v[48:51], v[76:79], a[140:143]\n"
      "v_add_f32 v112, v112, v4\n"
      "v_mfma_f32_16x16x32_bf16 a[136:139], v[48:51], v[72:75], a[136:139]\n"
      "s_add_u32 s36, s36, 16384\n"
      "ds_read_b64_tr_b16 v[90:91], v117 offset:26112\n"
      "v_mfma_f32_16x16x32_bf16 v[200:203], v[40:43], v[72:75], v[200:203]\n"
      "v_exp_f32 v5, v5\n"
      "v_mfma_f32_16x16x32_bf16 v[204:207], v[40:43], v[76:79], v[204:207]\n"
      "ds_read_b64_tr_b16 v[92:93], v118 offset:17920\n"
      "v_mfma_f32_16x16x32_bf16 v[140:143], v[32:35], v[76:79], v[140:143]\n"
      "v_add_f32 v112, v112, v5\n"
      "v_mfma_f32_16x16x32_bf16 v[136:139], v[32:35], v[72:75], v[136:139]\n"
      "s_waitcnt lgkmcnt(3)\n"
      "ds_read_b64_tr_b16 v[94:95], v118 offset:26112\n"
      "v_cvt_pk_bf16_f32 v2, v4, v5\n"
      "v_mfma_f32_16x16x32_bf16 v[144:147], v[32:35], v[80:83], v[144:147]\n"
      "v_exp_f32 v6, v6\n"
      "v_mfma_f32_16x16x32_bf16 v[148:151], v[32:35], v[84:87], v[148:151]\n"
      "ds_read_b64_tr_b16 v[64:65], v117 offset:18432\n"
      "v_mfma_f32_16x16x32_bf16 v[212:215], v[40:43], v[84:87], v[212:215]\n"
      "ds_read_b64_tr_b16 v[66:67], v117 offset:26624\n"
      "v_add_f32 v112, v112, v6\n"
      "v_mfma_f32_16x16x32_bf16 v[208:211], v[40:43], v[80:83], v[208:211]\n"
      "ds_read_b64_tr_b16 v[68:69], v118 offset:18432\n"
      "v_mfma_f32_16x16x32_bf16 a[144:147], v[48:51], v[80:83], a[144:147]\n"
      "v_exp_f32 v7, v7\n"
      "v_mfma_f32_16x16x32_bf16 a[148:151], v[48:51], v[84:87], a[148:151]\n"
      "ds_read_b64_tr_b16 v[70:71], v118 offset:26624\n"
      "v_mfma_f32_16x16x32_bf16 a[212:215], v[56:59], v[84:87], a[212:215]\n"
      "s_add_u32 m0, s92, 66432\n"
      "v_add_f32 v112, v112, v7\n"
      "v_cvt_pk_bf16_f32 v3, v6, v7\n"
      "v_mfma_f32_16x16x32_bf16 a[208:211], v[56:59], v[80:83], a[208:211]\n"
      "s_waitcnt lgkmcnt(4)\n"
      "buffer_load_dwordx4 v120, s[24:27], s37 offen offset:128 nt lds\n"
      "v_mfma_f32_16x16x32_bf16 a[216:219], v[56:59], v[88:91], a[216:219]\n"
      "v_exp_f32 v8, v8\n"
      "v_mfma_f32_16x16x32_bf16 a[220:223], v[56:59], v[92:95], a[220:223]\n"
      "ds_read_b64_tr_b16 v[72:73], v117 offset:18944\n"
      "v_mfma_f32_16x16x32_bf16 a[156:159], v[48:51], v[92:95], a[156:159]\n"
      "v_add_f32 v113, v113, v8\n"
      "v_mfma_f32_16x16x32_bf16 a[152:155], v[48:51], v[88:91], a[152:155]\n"
      "ds_read_b64_tr_b16 v[74:75], v117 offset:27136\n"
      "v_mfma_f32_16x16x32_bf16 v[216:219], v[40:43], v[88:91], v[216:219]\n"
      "v_exp_f32 v9, v9\n"
      "v_mfma_f32_16x16x32_bf16 v[220:223], v[40:43], v[92:95], v[220:223]\n"
      "ds_read_b64_tr_b16 v[76:77], v118 offset:18944\n"
      "v_mfma_f32_16x16x32_bf16 v[156:159], v[32:35], v[92:95], v[156:159]\n"
      "v_add_f32 v113, v113, v9\n"
      "v_mfma_f32_16x16x32_bf16 v[152:155], v[32:35], v[88:91], v[152:155]\n"
      "s_waitcnt lgkmcnt(3)\n"
      "ds_read_b64_tr_b16 v[78:79], v118 offset:27136\n"
      "v_cvt_pk_bf16_f32 v8, v8, v9\n"
      "v_mfma_f32_16x16x32_bf16 v[160:163], v[32:35], v[64:67], v[160:163]\n"
      "v_exp_f32 v10, v10\n"
      "v_mfma_f32_16x16x32_bf16 v[164:167], v[32:35], v[68:71], v[164:167]\n"
      "ds_read_b64_tr_b16 v[80:81], v117 offset:19456\n"
      "v_mfma_f32_16x16x32_bf16 v[228:231], v[40:43], v[68:71], v[228:231]\n"
      "ds_read_b64_tr_b16 v[82:83], v117 offset:27648\n"
      "v_add_f32 v113, v113, v10\n"
      "v_mfma_f32_16x16x32_bf16 v[224:227], v[40:43], v[64:67], v[224:227]\n"
      "ds_read_b64_tr_b16 v[84:85], v118 offset:19456\n"
      "v_mfma_f32_16x16x32_bf16 a[160:163], v[48:51], v[64:67], a[160:163]\n"
      "v_exp_f32 v11, v11\n"
      "v_mfma_f32_16x16x32_bf16 a[164:167], v[48:51], v[68:71], a[164:167]\n"
      "ds_read_b64_tr_b16 v[86:87], v118 offset:27648\n"
      "v_mfma_f32_16x16x32_bf16 a[228:231], v[56:59], v[68:71], a[228:231]\n"
      "s_add_u32 m0, s92, 67328\n"
      "v_add_f32 v113, v113, v11\n"
      "v_cvt_pk_bf16_f32 v9, v10, v11\n"
      "v_mfma_f32_16x16x32_bf16 a[224:227], v[56:59], v[64:67], a[224:227]\n"
      "s_waitcnt lgkmcnt(4)\n"
      "buffer_load_dwordx4 v120, s[24:27], s37 offen offset:256 nt lds\n"
      "v_mfma_f32_16x16x32_bf16 a[232:235], v[56:59], v[72:75], a[232:235]\n"
      "v_exp_f32 v12, v12\n"
      "v_mfma_f32_16x16x32_bf16 a[236:239], v[56:59], v[76:79], a[236:239]\n"
      "ds_read_b64_tr_b16 v[88:89], v117 offset:19968\n"
      "v_mfma_f32_16x16x32_bf16 a[172:175], v[48:51], v[76:79], a[172:175]\n"
      "v_add_f32 v113, v113, v12\n"
      "v_mfma_f32_16x16x32_bf16 a[168:171], v[48:51], v[72:75], a[168:171]\n"
      "ds_read_b64_tr_b16 v[90:91], v117 offset:28160\n"
      "v_mfma_f32_16x16x32_bf16 v[232:235], v[40:43], v[72:75], v[232:235]\n"
      "v_exp_f32 v13, v13\n"
      "v_mfma_f32_16x16x32_bf16 v[236:239], v[40:43], v[76:79], v[236:239]\n"
      "ds_read_b64_tr_b16 v[92:93], v118 offset:19968\n"
      "v_mfma_f32_16x16x32_bf16 v[172:175], v[32:35], v[76:79], v[172:175]\n"
      "v_add_f32 v113, v113, v13\n"
      "v_mfma_f32_16x16x32_bf16 v[168:171], v[32:35], v[72:75], v[168:171]\n"
      "s_waitcnt lgkmcnt(3)\n"
      "ds_read_b64_tr_b16 v[94:95], v118 offset:28160\n"
      "v_cvt_pk_bf16_f32 v10, v12, v13\n"
      "v_mfma_f32_16x16x32_bf16 v[176:179], v[32:35], v[80:83], v[176:179]\n"
      "v_exp_f32 v14, v14\n"
      "v_mfma_f32_16x16x32_bf16 v[180:183], v[32:35], v[84:87], v[180:183]\n"
      "ds_read_b128 v[64:67], v116 offset:49152\n"
      "v_mfma_f32_16x16x32_bf16 v[244:247], v[40:43], v[84:87], v[244:247]\n"
      "v_add_f32 v113, v113, v14\n"
      "v_mfma_f32_16x16x32_bf16 v[240:243], v[40:43], v[80:83], v[240:243]\n"
      "ds_read_b128 v[68:71], v116 offset:49664\n"
      "v_mfma_f32_16x16x32_bf16 a[176:179], v[48:51], v[80:83], a[176:179]\n"
      "v_exp_f32 v15, v15\n"
      "v_mfma_f32_16x16x32_bf16 a[180:183], v[48:51], v[84:87], a[180:183]\n"
      "v_mfma_f32_16x16x32_bf16 a[244:247], v[56:59], v[84:87], a[244:247]\n"
      "s_add_u32 m0, s92, 68224\n"
      "v_add_f32 v113, v113, v15\n"
      "v_cvt_pk_bf16_f32 v11, v14, v15\n"
      "v_mfma_f32_16x16x32_bf16 a[240:243], v[56:59], v[80:83], a[240:243]\n"
      "s_waitcnt lgkmcnt(2)\n"
      "buffer_load_dwordx4 v120, s[24:27], s37 offen offset:384 nt lds\n"
      "v_mfma_f32_16x16x32_bf16 a[248:251], v[56:59], v[88:91], a[248:251]\n"
      "v_exp_f32 v16, v16\n"
      "v_mfma_f32_16x16x32_bf16 a[252:255], v[56:59], v[92:95], a[252:255]\n"
      "s_add_u32 s37, s37, 16384\n"
      "ds_read_b128 v[72:75], v116 offset:50176\n"
      "v_mfma_f32_16x16x32_bf16 a[188:191], v[48:51], v[92:95], a[188:191]\n"
      "v_add_f32 v114, v114, v16\n"
      "v_mfma_f32_16x16x32_bf16 a[184:187], v[48:51], v[88:91], a[184:187]\n"
      "s_sub_u32 s39, s39, 1\n"
      "v_mfma_f32_16x16x32_bf16 v[248:251], v[40:43], v[88:91], v[248:251]\n"
      "v_exp_f32 v17, v17\n"
      "v_mfma_f32_16x16x32_bf16 v[252:255], v[40:43], v[92:95], v[252:255]\n"
      "s_cmp_lg_u32 s39, 0\n"
      "ds_read_b128 v[76:79], v116 offset:50688\n"
      "v_mfma_f32_16x16x32_bf16 v[188:191], v[32:35], v[92:95], v[188:191]\n"
      "v_add_f32 v114, v114, v17\n"
      "v_mfma_f32_16x16x32_bf16 v[184:187], v[32:35], v[88:91], v[184:187]\n"
      "s_waitcnt lgkmcnt(2)\n"
      "v_cvt_pk_bf16_f32 v16, v16, v17\n"
      "s_cbranch_scc1 LOOP%=\n"
      "s_branch TAIL%=\n"
      "LATE4_%=:\n"
      "v_mbcnt_lo_u32_b32 v121, -1, 0\n"
      "v_mbcnt_hi_u32_b32 v121, -1, v121\n"
      "v_lshlrev_b32 v121, 4, v121\n"
      "global_load_dwordx4 v[122:125], v121, s[62:63] offset:0\n"
      "global_load_dwordx4 v[122:125], v121, s[62:63] offset:1024\n"
      "global_load_dwordx4 v[122:125], v121, s[62:63] offset:2048\n"
      "s_branch BACK4_%=\n"
      "LATE5_%=:\n"
      "global_load_dwordx4 v[122:125], v121, s[62:63] offset:3072\n"
      "s_add_u32 s62, s62, 4096\n"
      "s_addc_u32 s63, s63, 0\n"
      "global_load_dwordx4 v[122:125], v121, s[62:63] offset:0\n"
      "s_branch BACK5_%=\n"
      "TAIL%=:\n"
      "v_mfma_f32_16x16x32_bf16 v[32:35], v[64:67], a[0:3], v[96:99]\n"
      "v_exp_f32 v18, v18\n"
      "v_mfma_f32_16x16x32_bf16 v[32:35], v[68:71], a[4:7], v[32:35]\n"
      "ds_read_b128 v[80:83], v116 offset:51200\n"
      "v_mfma_f32_16x16x32_bf16 v[40:43], v[68:71], a[36:39], v[100:103]\n"
      "v_add_f32 v114, v114, v18\n"
      "v_mfma_f32_16x16x32_bf16 v[40:43], v[64:67], a[32:35], v[40:43]\n"
      "ds_read_b128 v[84:87], v116 offset:51712\n"
      "v_mfma_f32_16x16x32_bf16 v[48:51], v[64:67], a[64:67], v[104:107]\n"
      "s_waitcnt vmcnt(12)\n"
      "s_barrier\n"
      "v_exp_f32 v19, v19\n"
      "v_mfma_f32_16x16x32_bf16 v[48:51], v[68:71], a[68:71], v[48:51]\n"
      "s_mov_b32 s52, 491520\n"
      "v_mfma_f32_16x16x32_bf16 v[56:59], v[68:71], a[100:103], v[108:111]\n"
      "s_add_u32 m0, s92, 32768\n"
      "v_add_f32 v114, v114, v19\n"
      "v_cvt_pk_bf16_f32 v17, v18, v19\n"
      "v_mfma_f32_16x16x32_bf16 v[56:59], v[64:67], a[96:99], v[56:59]\n"
      "s_waitcnt lgkmcnt(2)\n"
      "buffer_load_dwordx4 v119, s[20:23], s52 offen offset:0 nt lds\n"
      "v_mfma_f32_16x16x32_bf16 v[56:59], v[72:75], a[104:107], v[56:59]\n"
      "v_exp_f32 v20, v20\n"
      "v_mfma_f32_16x16x32_bf16 v[56:59], v[76:79], a[108:111], v[56:59]\n"
      "ds_read_b128 v[88:91], v116 offset:52224\n"
      "v_mfma_f32_16x16x32_bf16 v[48:51], v[76:79], a[76:79], v[48:51]\n"
      "v_add_f32 v114, v114, v20\n"
      "v_mfma_f32_16x16x32_bf16 v[48:51], v[72:75], a[72:75], v[48:51]\n"
      "ds_read_b128 v[92:95], v116 offset:52736\n"
      "v_mfma_f32_16x16x32_bf16 v[40:43], v[72:75], a[40:43], v[40:43]\n"
      "v_exp_f32 v21, v21\n"
      "v_mfma_f32_16x16x32_bf16 v[40:43], v[76:79], a[44:47], v[40:43]\n"
      "v_mfma_f32_16x16x32_bf16 v[32:35], v[76:79], a[12:15], v[32:35]\n"
      "s_add_u32 m0, s92, 33664\n"
      "v_add_f32 v114, v114, v21\n"
      "v_cvt_pk_bf16_f32 v18, v20, v21\n"
      "v_mfma_f32_16x16x32_bf16 v[32:35], v[72:75], a[8:11], v[32:35]\n"
      "s_waitcnt lgkmcnt(2)\n"
      "buffer_load_dwordx4 v119, s[20:23], s52 offen offset:128 nt lds\n"
      "v_mfma_f32_16x16x32_bf16 v[32:35], v[80:83], a[16:19], v[32:35]\n"
      "v_exp_f32 v22, v22\n"
      "v_mfma_f32_16x16x32_bf16 v[32:35], v[84:87], a[20:23], v[32:35]\n"
      "ds_read_b128 v[64:67], v116 offset:57344\n"
      "v_mfma_f32_16x16x32_bf16 v[40:43], v[84:87], a[52:55], v[40:43]\n"
      "v_add_f32 v114, v114, v22\n"
      "v_mfma_f32_16x16x32_bf16 v[40:43], v[80:83], a[48:51], v[40:43]\n"
      "ds_read_b128 v[68:71], v116 offset:57856\n"
      "v_mfma_f32_16x16x32_bf16 v[48:51], v[80:83], a[80:83], v[48:51]\n"
      "v_exp_f32 v23, v23\n"
      "v_mfma_f32_16x16x32_bf16 v[48:51], v[84:87], a[84:87], v[48:51]\n"
      "v_mfma_f32_16x16x32_bf16 v[56:59], v[84:87], a[116:119], v[56:59]\n"
      "s_add_u32 m0, s92, 34560\n"
      "v_add_f32 v114, v114, v23\n"
      "v_cvt_pk_bf16_f32 v19, v22, v23\n"
      "v_mfma_f32_16x16x32_bf16 v[56:59], v[80:83], a[112:115], v[56:59]\n"
      "s_waitcnt lgkmcnt(2)\n"
      "buffer_load_dwordx4 v119, s[20:23], s52 offen offset:256 nt lds\n"
      "v_mfma_f32_16x16x32_bf16 v[56:59], v[88:91], a[120:123], v[56:59]\n"
      "v_exp_f32 v24, v24\n"
      "v_mfma_f32_16x16x32_bf16 v[56:59], v[92:95], a[124:127], v[56:59]\n"
      "ds_read_b128 v[72:75], v116 offset:58368\n"
      "v_mfma_f32_16x16x32_bf16 v[48:51], v[92:95], a[92:95], v[48:51]\n"
      "v_add_f32 v115, v115, v24\n"
      "v_mfma_f32_16x16x32_bf16 v[48:51], v[88:91], a[88:91], v[48:51]\n"
      "ds_read_b128 v[76:79], v116 offset:58880\n"
      "v_mfma_f32_16x16x32_bf16 v[40:43], v[88:91], a[56:59], v[40:43]\n"
      "v_exp_f32 v25, v25\n"
      "v_mfma_f32_16x16x32_bf16 v[40:43], v[92:95], a[60:63], v[40:43]\n"
      "v_mfma_f32_16x16x32_bf16 v[32:35], v[92:95], a[28:31], v[32:35]\n"
      "s_add_u32 m0, s92, 35456\n"
      "v_add_f32 v115, v115, v25\n"
      "v_cvt_pk_bf16_f32 v24, v24, v25\n"
      "v_mfma_f32_16x16x32_bf16 v[32:35], v[88:91], a[24:27], v[32:35]\n"
      "s_waitcnt lgkmcnt(2)\n"
      "buffer_load_dwordx4 v119, s[20:23], s52 offen offset:384 nt lds\n"
      "v_mfma_f32_16x16x32_bf16 v[36:39], v[64:67], a[0:3], v[96:99]\n"
      "v_exp_f32 v26, v26\n"
      "v_mfma_f32_16x16x32_bf16 v[36:39], v[68:71], a[4:7], v[36:39]\n"
      "ds_read_b128 v[80:83], v116 offset:59392\n"
      "v_mfma_f32_16x16x32_bf16 v[44:47], v[68:71], a[36:39], v[100:103]\n"
      "v_add_f32 v115, v115, v26\n"
      "v_mfma_f32_16x16x32_bf16 v[44:47], v[64:67], a[32:35], v[44:47]\n"
      "ds_read_b128 v[84:87], v116 offset:59904\n"
      "v_mfma_f32_16x16x32_bf16 v[52:55], v[64:67], a[64:67], v[104:107]\n"
      "v_exp_f32 v27, v27\n"
      "v_mfma_f32_16x16x32_bf16 v[52:55], v[68:71], a[68:71], v[52:55]\n"
      "s_mov_b32 s52, 507904\n"
      "v_mfma_f32_16x16x32_bf16 v[60:63], v[68:71], a[100:103], v[108:111]\n"
      "s_add_u32 m0, s92, 147456\n"
      "v_add_f32 v115, v115, v27\n"
      "v_cvt_pk_bf16_f32 v25, v26, v27\n"
      "v_mfma_f32_16x16x32_bf16 v[60:63], v[64:67], a[96:99], v[60:63]\n"
      "s_waitcnt lgkmcnt(2)\n"
      "buffer_load_dwordx4 v120, s[24:27], s52 offen offset:0 nt lds\n"
      "v_mfma_f32_16x16x32_bf16 v[60:63], v[72:75], a[104:107], v[60:63]\n"
      "v_exp_f32 v28, v28\n"
      "v_mfma_f32_16x16x32_bf16 v[60:63], v[76:79], a[108:111], v[60:63]\n"
      "ds_read_b128 v[88:91], v116 offset:60416\n"
      "v_mfma_f32_16x16x32_bf16 v[52:55], v[76:79], a[76:79], v[52:55]\n"
      "v_add_f32 v115, v115, v28\n"
      "v_mfma_f32_16x16x32_bf16 v[52:55], v[72:75], a[72:75], v[52:55]\n"
      "ds_read_b128 v[92:95], v116 offset:60928\n"
      "v_mfma_f32_16x16x32_bf16 v[44:47], v[72:75], a[40:43], v[44:47]\n"
      "v_exp_f32 v29, v29\n"
      "v_mfma_f32_16x16x32_bf16 v[44:47], v[76:79], a[44:47], v[44:47]\n"
      "v_mfma_f32_16x16x32_bf16 v[36:39], v[76:79], a[12:15], v[36:39]\n"
      "s_add_u32 m0, s92, 148352\n"
      "v_add_f32 v115, v115, v29\n"
      "v_cvt_pk_bf16_f32 v26, v28, v29\n"
      "v_mfma_f32_16x16x32_bf16 v[36:39], v[72:75], a[8:11], v[36:39]\n"
      "s_waitcnt lgkmcnt(2)\n"
      "buffer_load_dwordx4 v120, s[24:27], s52 offen offset:128 nt lds\n"
      "v_mfma_f32_16x16x32_bf16 v[36:39], v[80:83], a[16:19], v[36:39]\n"
      "v_exp_f32 v30, v30\n"
      "v_mfma_f32_16x16x32_bf16 v[36:39], v[84:87], a[20:23], v[36:39]\n"
      "ds_read_b64_tr_b16 v[64:65], v117 offset:32768\n"
      "v_mfma_f32_16x16x32_bf16 v[44:47], v[84:87], a[52:55], v[44:47]\n"
      "ds_read_b64_tr_b16 v[66:67], v117 offset:40960\n"
      "v_add_f32 v115, v115, v30\n"
      "v_mfma_f32_16x16x32_bf16 v[44:47], v[80:83], a[48:51], v[44:47]\n"
      "ds_read_b64_tr_b16 v[68:69], v118 offset:32768\n"
      "v_mfma_f32_16x16x32_bf16 v[52:55], v[80:83], a[80:83], v[52:55]\n"
      "v_exp_f32 v31, v31\n"
      "v_mfma_f32_16x16x32_bf16 v[52:55], v[84:87], a[84:87], v[52:55]\n"
      "ds_read_b64_tr_b16 v[70:71], v118 offset:40960\n"
      "v_mfma_f32_16x16x32_bf16 v[60:63], v[84:87], a[116:119], v[60:63]\n"
      "s_add_u32 m0, s92, 149248\n"
      "v_add_f32 v115, v115, v31\n"
      "v_cvt_pk_bf16_f32 v27, v30, v31\n"
      "v_mfma_f32_16x16x32_bf16 v[60:63], v[80:83], a[112:115], v[60:63]\n"
      "s_waitcnt lgkmcnt(4)\n"
      "buffer_load_dwordx4 v120, s[24:27], s52 offen offset:256 nt lds\n"
      "v_mfma_f32_16x16x32_bf16 v[60:63], v[88:91], a[120:123], v[60:63]\n"
      "v_exp_f32 v32, v32\n"
      "v_mfma_f32_16x16x32_bf16 v[60:63], v[92:95], a[124:127], v[60:63]\n"
      "ds_read_b64_tr_b16 v[72:73], v117 offset:33280\n"
      "v_mfma_f32_16x16x32_bf16 v[52:55], v[92:95], a[92:95], v[52:55]\n"
      "ds_read_b64_tr_b16 v[74:75], v117 offset:41472\n"
      "v_add_f32 v112, v112, v32\n"
      "v_mfma_f32_16x16x32_bf16 v[52:55], v[88:91], a[88:91], v[52:55]\n"
      "ds_read_b64_tr_b16 v[76:77], v118 offset:33280\n"
      "v_mfma_f32_16x16x32_bf16 v[44:47], v[88:91], a[56:59], v[44:47]\n"
      "s_barrier\n"
      "v_exp_f32 v33, v33\n"
      "v_mfma_f32_16x16x32_bf16 v[44:47], v[92:95], a[60:63], v[44:47]\n"
      "s_mov_b32 s52, 507904\n"
      "ds_read_b64_tr_b16 v[78:79], v118 offset:41472\n"
      "v_mfma_f32_16x16x32_bf16 v[36:39], v[92:95], a[28:31], v[36:39]\n"
      "s_add_u32 m0, s92, 49152\n"
      "v_add_f32 v112, v112, v33\n"
      "v_cvt_pk_bf16_f32 v32, v32, v33\n"
      "v_mfma_f32_16x16x32_bf16 v[36:39], v[88:91], a[24:27], v[36:39]\n"
      "s_waitcnt lgkmcnt(4)\n"
      "buffer_load_dwordx4 v119, s[20:23], s52 offen offset:0 nt lds\n"
      "v_mfma_f32_16x16x32_bf16 v[128:131], v[0:3], v[64:67], v[128:131]\n"
      "v_exp_f32 v34, v34\n"
      "v_mfma_f32_16x16x32_bf16 v[132:135], v[0:3], v[68:71], v[132:135]\n"
      "ds_read_b64_tr_b16 v[80:81], v117 offset:33792\n"
      "v_mfma_f32_16x16x32_bf16 v[196:199], v[8:11], v[68:71], v[196:199]\n"
      "ds_read_b64_tr_b16 v[82:83], v117 offset:41984\n"
      "v_add_f32 v112, v112, v34\n"
      "v_mfma_f32_16x16x32_bf16 v[192:195], v[8:11], v[64:67], v[192:195]\n"
      "ds_read_b64_tr_b16 v[84:85], v118 offset:33792\n"
      "v_mfma_f32_16x16x32_bf16 a[128:131], v[16:19], v[64:67], a[128:131]\n"
      "v_exp_f32 v35, v35\n"
      "v_mfma_f32_16x16x32_bf16 a[132:135], v[16:19], v[68:71], a[132:135]\n"
      "ds_read_b64_tr_b16 v[86:87], v118 offset:41984\n"
      "v_mfma_f32_16x16x32_bf16 a[196:199], v[24:27], v[68:71], a[196:199]\n"
      "s_add_u32 m0, s92, 50048\n"
      "v_add_f32 v112, v112, v35\n"
      "v_cvt_pk_bf16_f32 v33, v34, v35\n"
      "v_mfma_f32_16x16x32_bf16 a[192:195], v[24:27], v[64:67], a[192:195]\n"
      "s_waitcnt lgkmcnt(4)\n"
      "buffer_load_dwordx4 v119, s[20:23], s52 offen offset:128 nt lds\n"
      "v_mfma_f32_16x16x32_bf16 a[200:203], v[24:27], v[72:75], a[200:203]\n"
      "v_exp_f32 v36, v36\n"
      "v_mfma_f32_16x16x32_bf16 a[204:207], v[24:27], v[76:79], a[204:207]\n"
      "ds_read_b64_tr_b16 v[88:89], v117 offset:34304\n"
      "v_mfma_f32_16x16x32_bf16 a[140:143], v[16:19], v[76:79], a[140:143]\n"
      "ds_read_b64_tr_b16 v[90:91], v117 offset:42496\n"
      "v_add_f32 v112, v112, v36\n"
      "v_mfma_f32_16x16x32_bf16 a[136:139], v[16:19], v[72:75], a[136:139]\n"
      "ds_read_b64_tr_b16 v[92:93], v118 offset:34304\n"
      "v_mfma_f32_16x16x32_bf16 v[200:203], v[8:11], v[72:75], v[200:203]\n"
      "v_exp_f32 v37, v37\n"
      "v_mfma_f32_16x16x32_bf16 v[204:207], v[8:11], v[76:79], v[204:207]\n"
      "ds_read_b64_tr_b16 v[94:95], v118 offset:42496\n"
      "v_mfma_f32_16x16x32_bf16 v[140:143], v[0:3], v[76:79], v[140:143]\n"
      "s_add_u32 m0, s92, 50944\n"
      "v_add_f32 v112, v112, v37\n"
      "v_cvt_pk_bf16_f32 v34, v36, v37\n"
      "v_mfma_f32_16x16x32_bf16 v[136:139], v[0:3], v[72:75], v[136:139]\n"
      "s_waitcnt lgkmcnt(4)\n"
      "buffer_load_dwordx4 v119, s[20:23], s52 offen offset:256 nt lds\n"
      "v_mfma_f32_16x16x32_bf16 v[144:147], v[0:3], v[80:83], v[144:147]\n"
      "v_exp_f32 v38, v38\n"
      "v_mfma_f32_16x16x32_bf16 v[148:151], v[0:3], v[84:87], v[148:151]\n"
      "ds_read_b64_tr_b16 v[64:65], v117 offset:34816\n"
      "v_mfma_f32_16x16x32_bf16 v[212:215], v[8:11], v[84:87], v[212:215]\n"
      "ds_read_b64_tr_b16 v[66:67], v117 offset:43008\n"
      "v_add_f32 v112, v112, v38\n"
      "v_mfma_f32_16x16x32_bf16 v[208:211], v[8:11], v[80:83], v[208:211]\n"
      "ds_read_b64_tr_b16 v[68:69], v118 offset:34816\n"
      "v_mfma_f32_16x16x32_bf16 a[144:147], v[16:19], v[80:83], a[144:147]\n"
      "v_exp_f32 v39, v39\n"
      "v_mfma_f32_16x16x32_bf16 a[148:151], v[16:19], v[84:87], a[148:151]\n"
      "ds_read_b64_tr_b16 v[70:71], v118 offset:43008\n"
      "v_mfma_f32_16x16x32_bf16 a[212:215], v[24:27], v[84:87], a[212:215]\n"
      "s_add_u32 m0, s92, 51840\n"
      "v_add_f32 v112, v112, v39\n"
      "v_cvt_pk_bf16_f32 v35, v38, v39\n"
      "v_mfma_f32_16x16x32_bf16 a[208:211], v[24:27], v[80:83], a[208:211]\n"
      "s_waitcnt lgkmcnt(4)\n"
      "buffer_load_dwordx4 v119, s[20:23], s52 offen offset:384 nt lds\n"
      "v_mfma_f32_16x16x32_bf16 a[216:219], v[24:27], v[88:91], a[216:219]\n"
      "v_exp_f32 v40, v40\n"
      "v_mfma_f32_16x16x32_bf16 a[220:223], v[24:27], v[92:95], a[220:223]\n"
      "ds_read_b64_tr_b16 v[72:73], v117 offset:35328\n"
      "v_mfma_f32_16x16x32_bf16 a[156:159], v[16:19], v[92:95], a[156:159]\n"
      "ds_read_b64_tr_b16 v[74:75], v117 offset:43520\n"
      "v_add_f32 v113, v113, v40\n"
      "v_mfma_f32_16x16x32_bf16 a[152:155], v[16:19], v[88:91], a[152:155]\n"
      "ds_read_b64_tr_b16 v[76:77], v118 offset:35328\n"
      "v_mfma_f32_16x16x32_bf16 v[216:219], v[8:11], v[88:91], v[216:219]\n"
      "v_exp_f32 v41, v41\n"
      "v_mfma_f32_16x16x32_bf16 v[220:223], v[8:11], v[92:95], v[220:223]\n"
      "s_mov_b32 s52, 507904\n"
      "ds_read_b64_tr_b16 v[78:79], v118 offset:43520\n"
      "v_mfma_f32_16x16x32_bf16 v[156:159], v[0:3], v[92:95], v[156:159]\n"
      "s_add_u32 m0, s92, 150144\n"
      "v_add_f32 v113, v113, v41\n"
      "v_cvt_pk_bf16_f32 v40, v40, v41\n"
      "v_mfma_f32_16x16x32_bf16 v[152:155], v[0:3], v[88:91], v[152:155]\n"
      "s_waitcnt lgkmcnt(4)\n"
      "buffer_load_dwordx4 v120, s[24:27], s52 offen offset:384 nt lds\n"
      "v_mfma_f32_16x16x32_bf16 v[160:163], v[0:3], v[64:67], v[160:163]\n"
      "v_exp_f32 v42, v42\n"
      "v_mfma_f32_16x16x32_bf16 v[164:167], v[0:3], v[68:71], v[164:167]\n"
      "ds_read_b64_tr_b16 v[80:81], v117 offset:35840\n"
      "v_mfma_f32_16x16x32_bf16 v[228:231], v[8:11], v[68:71], v[228:231]\n"
      "ds_read_b64_tr_b16 v[82:83], v117 offset:44032\n"
      "v_add_f32 v113, v113, v42\n"
      "v_mfma_f32_16x16x32_bf16 v[224:227], v[8:11], v[64:67], v[224:227]\n"
      "ds_read_b64_tr_b16 v[84:85], v118 offset:35840\n"
      "v_mfma_f32_16x16x32_bf16 a[160:163], v[16:19], v[64:67], a[160:163]\n"
      "v_exp_f32 v43, v43\n"
      "v_mfma_f32_16x16x32_bf16 a[164:167], v[16:19], v[68:71], a[164:167]\n"
      "s_mov_b32 s52, 475136\n"
      "ds_read_b64_tr_b16 v[86:87], v118 offset:44032\n"
      "v_mfma_f32_16x16x32_bf16 a[228:231], v[24:27], v[68:71], a[228:231]\n"
      "s_add_u32 m0, s92, 81920\n"
      "v_add_f32 v113, v113, v43\n"
      "v_cvt_pk_bf16_f32 v41, v42, v43\n"
      "v_mfma_f32_16x16x32_bf16 a[224:227], v[24:27], v[64:67], a[224:227]\n"
      "s_waitcnt lgkmcnt(4)\n"
      "buffer_load_dwordx4 v120, s[24:27], s52 offen offset:0 nt lds\n"
      "v_mfma_f32_16x16x32_bf16 a[232:235], v[24:27], v[72:75], a[232:235]\n"
      "v_exp_f32 v44, v44\n"
      "v_mfma_f32_16x16x32_bf16 a[236:239], v[24:27], v[76:79], a[236:239]\n"
      "ds_read_b64_tr_b16 v[88:89], v117 offset:36352\n"
      "v_mfma_f32_16x16x32_bf16 a[172:175], v[16:19], v[76:79], a[172:175]\n"
      "ds_read_b64_tr_b16 v[90:91], v117 offset:44544\n"
      "v_add_f32 v113, v113, v44\n"
      "v_mfma_f32_16x16x32_bf16 a[168:171], v[16:19], v[72:75], a[168:171]\n"
      "ds_read_b64_tr_b16 v[92:93], v118 offset:36352\n"
      "v_mfma_f32_16x16x32_bf16 v[232:235], v[8:11], v[72:75], v[232:235]\n"
      "v_exp_f32 v45, v45\n"
      "v_mfma_f32_16x16x32_bf16 v[236:239], v[8:11], v[76:79], v[236:239]\n"
      "ds_read_b64_tr_b16 v[94:95], v118 offset:44544\n"
      "v_mfma_f32_16x16x32_bf16 v[172:175], v[0:3], v[76:79], v[172:175]\n"
      "s_add_u32 m0, s92, 82816\n"
      "v_add_f32 v113, v113, v45\n"
      "v_cvt_pk_bf16_f32 v42, v44, v45\n"
      "v_mfma_f32_16x16x32_bf16 v[168:171], v[0:3], v[72:75], v[168:171]\n"
      "s_waitcnt lgkmcnt(4)\n"
      "buffer_load_dwordx4 v120, s[24:27], s52 offen offset:128 nt lds\n"
      "v_mfma_f32_16x16x32_bf16 v[176:179], v[0:3], v[80:83], v[176:179]\n"
      "v_exp_f32 v46, v46\n"
      "v_mfma_f32_16x16x32_bf16 v[180:183], v[0:3], v[84:87], v[180:183]\n"
      "ds_read_b128 v[64:67], v116 offset:0\n"
      "v_mfma_f32_16x16x32_bf16 v[244:247], v[8:11], v[84:87], v[244:247]\n"
      "v_add_f32 v113, v113, v46\n"
      "v_mfma_f32_16x16x32_bf16 v[240:243], v[8:11], v[80:83], v[240:243]\n"
      "ds_read_b128 v[68:71], v116 offset:512\n"
      "v_mfma_f32_16x16x32_bf16 a[176:179], v[16:19], v[80:83], a[176:179]\n"
      "v_exp_f32 v47, v47\n"
      "v_mfma_f32_16x16x32_bf16 a[180:183], v[16:19], v[84:87], a[180:183]\n"
      "v_mfma_f32_16x16x32_bf16 a[244:247], v[24:27], v[84:87], a[244:247]\n"
      "s_add_u32 m0, s92, 83712\n"
      "v_add_f32 v113, v113, v47\n"
      "v_cvt_pk_bf16_f32 v43, v46, v47\n"
      "v_mfma_f32_16x16x32_bf16 a[240:243], v[24:27], v[80:83], a[240:243]\n"
      "s_waitcnt lgkmcnt(2)\n"
      "buffer_load_dwordx4 v120, s[24:27], s52 offen offset:256 nt lds\n"
      "v_mfma_f32_16x16x32_bf16 a[248:251], v[24:27], v[88:91], a[248:251]\n"
      "v_exp_f32 v48, v48\n"
      "v_mfma_f32_16x16x32_bf16 a[252:255], v[24:27], v[92:95], a[252:255]\n"
      "ds_read_b128 v[72:75], v116 offset:1024\n"
      "v_mfma_f32_16x16x32_bf16 a[188:191], v[16:19], v[92:95], a[188:191]\n"
      "v_add_f32 v114, v114, v48\n"
      "v_mfma_f32_16x16x32_bf16 a[184:187], v[16:19], v[88:91], a[184:187]\n"
      "ds_read_b128 v[76:79], v116 offset:1536\n"
      "v_mfma_f32_16x16x32_bf16 v[248:251], v[8:11], v[88:91], v[248:251]\n"
      "v_exp_f32 v49, v49\n"
      "v_mfma_f32_16x16x32_bf16 v[252:255], v[8:11], v[92:95], v[252:255]\n"
      "v_mfma_f32_16x16x32_bf16 v[188:191], v[0:3], v[92:95], v[188:191]\n"
      "s_add_u32 m0, s92, 84608\n"
      "v_add_f32 v114, v114, v49\n"
      "v_cvt_pk_bf16_f32 v48, v48, v49\n"
      "v_mfma_f32_16x16x32_bf16 v[184:187], v[0:3], v[88:91], v[184:187]\n"
      "s_waitcnt lgkmcnt(2)\n"
      "buffer_load_dwordx4 v120, s[24:27], s52 offen offset:384 nt lds\n"
      "v_mfma_f32_16x16x32_bf16 v[0:3], v[64:67], a[0:3], v[96:99]\n"
      "ds_read_b128 v[80:83], v116 offset:2048\n"
      "ds_read_b128 v[88:91], v116 offset:3072\n"
      "v_mfma_f32_16x16x32_bf16 v[0:3], v[68:71], a[4:7], v[0:3]\n"
      "s_waitcnt vmcnt(20)\n"
      "s_barrier\n"
      "v_exp_f32 v50, v50\n"
      "v_mfma_f32_16x16x32_bf16 v[8:11], v[68:71], a[36:39], v[100:103]\n"
      "ds_read_b128 v[84:87], v116 offset:2560\n"
      "ds_read_b128 v[92:95], v116 offset:3584\n"
      "v_mfma_f32_16x16x32_bf16 v[8:11], v[64:67], a[32:35], v[8:11]\n"
      "s_waitcnt lgkmcnt(4)\n"
      "v_exp_f32 v51, v51\n"
      "v_mfma_f32_16x16x32_bf16 v[8:11], v[72:75], a[40:43], v[8:11]\n"
      "ds_read_b128 v[64:67], v116 offset:8192\n"
      "v_add_f32 v114, v114, v50\n"
      "v_add_f32 v114, v114, v51\n"
      "v_mfma_f32_16x16x32_bf16 v[8:11], v[76:79], a[44:47], v[8:11]\n"
      "v_exp_f32 v52, v52\n"
      "v_mfma_f32_16x16x32_bf16 v[0:3], v[76:79], a[12:15], v[0:3]\n"
      "ds_read_b128 v[68:71], v116 offset:8704\n"
      "v_cvt_pk_bf16_f32 v49, v50, v51\n"
      "v_mfma_f32_16x16x32_bf16 v[0:3], v[72:75], a[8:11], v[0:3]\n"
      "s_waitcnt lgkmcnt(3)\n"
      "v_exp_f32 v53, v53\n"
      "v_mfma_f32_16x16x32_bf16 v[0:3], v[80:83], a[16:19], v[0:3]\n"
      "ds_read_b128 v[72:75], v116 offset:9216\n"
      "v_add_f32 v114, v114, v52\n"
      "v_add_f32 v114, v114, v53\n"
      "v_mfma_f32_16x16x32_bf16 v[0:3], v[84:87], a[20:23], v[0:3]\n"
      "v_exp_f32 v54, v54\n"
      "v_mfma_f32_16x16x32_bf16 v[8:11], v[84:87], a[52:55], v[8:11]\n"
      "ds_read_b128 v[76:79], v116 offset:9728\n"
      "v_cvt_pk_bf16_f32 v50, v52, v53\n"
      "v_mfma_f32_16x16x32_bf16 v[8:11], v[80:83], a[48:51], v[8:11]\n"
      "s_waitcnt lgkmcnt(4)\n"
      "v_exp_f32 v55, v55\n"
      "v_mfma_f32_16x16x32_bf16 v[8:11], v[88:91], a[56:59], v[8:11]\n"
      "ds_read_b128 v[80:83], v116 offset:10240\n"
      "v_add_f32 v114, v114, v54\n"
      "v_add_f32 v114, v114, v55\n"
      "v_mfma_f32_16x16x32_bf16 v[8:11], v[92:95], a[60:63], v[8:11]\n"
      "v_exp_f32 v56, v56\n"
      "v_mfma_f32_16x16x32_bf16 v[0:3], v[92:95], a[28:31], v[0:3]\n"
      "ds_read_b128 v[84:87], v116 offset:10752\n"
      "v_cvt_pk_bf16_f32 v51, v54, v55\n"
      "v_mfma_f32_16x16x32_bf16 v[0:3], v[88:91], a[24:27], v[0:3]\n"
      "s_waitcnt lgkmcnt(4)\n"
      "v_exp_f32 v57, v57\n"
      "v_mfma_f32_16x16x32_bf16 v[4:7], v[64:67], a[0:3], v[96:99]\n"
      "ds_read_b128 v[88:91], v116 offset:11264\n"
      "v_add_f32 v115, v115, v56\n"
      "v_add_f32 v115, v115, v57\n"
      "v_mfma_f32_16x16x32_bf16 v[4:7], v[68:71], a[4:7], v[4:7]\n"
      "v_exp_f32 v58, v58\n"
      "v_mfma_f32_16x16x32_bf16 v[12:15], v[68:71], a[36:39], v[100:103]\n"
      "ds_read_b128 v[92:95], v116 offset:11776\n"
      "v_cvt_pk_bf16_f32 v56, v56, v57\n"
      "v_mfma_f32_16x16x32_bf16 v[12:15], v[64:67], a[32:35], v[12:15]\n"
      "s_waitcnt lgkmcnt(4)\n"
      "v_exp_f32 v59, v59\n"
      "v_mfma_f32_16x16x32_bf16 v[12:15], v[72:75], a[40:43], v[12:15]\n"
      "ds_read_b64_tr_b16 v[64:65], v117 offset:49152\n"
      "ds_read_b64_tr_b16 v[68:69], v118 offset:49152\n"
      "v_add_f32 v115, v115, v58\n"
      "v_mfma_f32_16x16x32_bf16 v[12:15], v[76:79], a[44:47], v[12:15]\n"
      "v_exp_f32 v60, v60\n"
      "v_mfma_f32_16x16x32_bf16 v[4:7], v[76:79], a[12:15], v[4:7]\n"
      "ds_read_b64_tr_b16 v[66:67], v117 offset:57344\n"
      "ds_read_b64_tr_b16 v[70:71], v118 offset:57344\n"
      "v_add_f32 v115, v115, v59\n"
      "v_mfma_f32_16x16x32_bf16 v[4:7], v[72:75], a[8:11], v[4:7]\n"
      "s_waitcnt lgkmcnt(6)\n"
      "v_exp_f32 v61, v61\n"
      "v_mfma_f32_16x16x32_bf16 v[4:7], v[80:83], a[16:19], v[4:7]\n"
      "ds_read_b64_tr_b16 v[72:73], v117 offset:49664\n"
      "ds_read_b64_tr_b16 v[76:77], v118 offset:49664\n"
      "v_cvt_pk_bf16_f32 v57, v58, v59\n"
      "v_mfma_f32_16x16x32_bf16 v[4:7], v[84:87], a[20:23], v[4:7]\n"
      "v_exp_f32 v62, v62\n"
      "v_mfma_f32_16x16x32_bf16 v[12:15], v[84:87], a[52:55], v[12:15]\n"
      "ds_read_b64_tr_b16 v[74:75], v117 offset:57856\n"
      "ds_read_b64_tr_b16 v[78:79], v118 offset:57856\n"
      "v_add_f32 v115, v115, v60\n"
      "v_mfma_f32_16x16x32_bf16 v[12:15], v[80:83], a[48:51], v[12:15]\n"
      "s_waitcnt lgkmcnt(8)\n"
      "v_exp_f32 v63, v63\n"
      "v_mfma_f32_16x16x32_bf16 v[12:15], v[88:91], a[56:59], v[12:15]\n"
      "ds_read_b64_tr_b16 v[80:81], v117 offset:50176\n"
      "v_add_f32 v115, v115, v61\n"
      "v_cvt_pk_bf16_f32 v58, v60, v61\n"
      "v_mfma_f32_16x16x32_bf16 v[12:15], v[92:95], a[60:63], v[12:15]\n"
      "ds_read_b64_tr_b16 v[84:85], v118 offset:50176\n"
      "v_add_f32 v115, v115, v62\n"
      "v_add_f32 v115, v115, v63\n"
      "v_mfma_f32_16x16x32_bf16 v[4:7], v[92:95], a[28:31], v[4:7]\n"
      "ds_read_b64_tr_b16 v[82:83], v117 offset:58368\n"
      "v_cvt_pk_bf16_f32 v59, v62, v63\n"
      "v_mfma_f32_16x16x32_bf16 v[4:7], v[88:91], a[24:27], v[4:7]\n"
      "s_waitcnt lgkmcnt(7)\n"
      "ds_read_b64_tr_b16 v[86:87], v118 offset:58368\n"
      "v_mfma_f32_16x16x32_bf16 v[128:131], v[32:35], v[64:67], v[128:131]\n"
      "v_mfma_f32_16x16x32_bf16 v[132:135], v[32:35], v[68:71], v[132:135]\n"
      "v_exp_f32 v0, v0\n"
      "v_mfma_f32_16x16x32_bf16 v[196:199], v[40:43], v[68:71], v[196:199]\n"
      "v_add_f32 v112, v112, v0\n"
      "v_mfma_f32_16x16x32_bf16 v[192:195], v[40:43], v[64:67], v[192:195]\n"
      "v_mfma_f32_16x16x32_bf16 a[128:131], v[48:51], v[64:67], a[128:131]\n"
      "v_mfma_f32_16x16x32_bf16 a[132:135], v[48:51], v[68:71], a[132:135]\n"
      "v_exp_f32 v1, v1\n"
      "v_mfma_f32_16x16x32_bf16 a[196:199], v[56:59], v[68:71], a[196:199]\n"
      "v_add_f32 v112, v112, v1\n"
      "v_cvt_pk_bf16_f32 v0, v0, v1\n"
      "v_mfma_f32_16x16x32_bf16 a[192:195], v[56:59], v[64:67], a[192:195]\n"
      "s_waitcnt lgkmcnt(4)\n"
      "v_mfma_f32_16x16x32_bf16 a[200:203], v[56:59], v[72:75], a[200:203]\n"
      "ds_read_b64_tr_b16 v[88:89], v117 offset:50688\n"
      "v_mfma_f32_16x16x32_bf16 a[204:207], v[56:59], v[76:79], a[204:207]\n"
      "v_exp_f32 v2, v2\n"
      "v_mfma_f32_16x16x32_bf16 a[140:143], v[48:51], v[76:79], a[140:143]\n"
      "ds_read_b64_tr_b16 v[90:91], v117 offset:58880\n"
      "v_add_f32 v112, v112, v2\n"
      "v_mfma_f32_16x16x32_bf16 a[136:139], v[48:51], v[72:75], a[136:139]\n"
      "v_mfma_f32_16x16x32_bf16 v[200:203], v[40:43], v[72:75], v[200:203]\n"
      "ds_read_b64_tr_b16 v[92:93], v118 offset:50688\n"
      "v_mfma_f32_16x16x32_bf16 v[204:207], v[40:43], v[76:79], v[204:207]\n"
      "v_exp_f32 v3, v3\n"
      "v_mfma_f32_16x16x32_bf16 v[140:143], v[32:35], v[76:79], v[140:143]\n"
      "ds_read_b64_tr_b16 v[94:95], v118 offset:58880\n"
      "v_add_f32 v112, v112, v3\n"
      "v_mfma_f32_16x16x32_bf16 v[136:139], v[32:35], v[72:75], v[136:139]\n"
      "s_waitcnt lgkmcnt(4)\n"
      "v_cvt_pk_bf16_f32 v1, v2, v3\n"
      "v_mfma_f32_16x16x32_bf16 v[144:147], v[32:35], v[80:83], v[144:147]\n"
      "ds_read_b64_tr_b16 v[64:65], v117 offset:51200\n"
      "v_mfma_f32_16x16x32_bf16 v[148:151], v[32:35], v[84:87], v[148:151]\n"
      "v_exp_f32 v4, v4\n"
      "v_mfma_f32_16x16x32_bf16 v[212:215], v[40:43], v[84:87], v[212:215]\n"
      "ds_read_b64_tr_b16 v[66:67], v117 offset:59392\n"
      "v_add_f32 v112, v112, v4\n"
      "v_mfma_f32_16x16x32_bf16 v[208:211], v[40:43], v[80:83], v[208:211]\n"
      "v_mfma_f32_16x16x32_bf16 a[144:147], v[48:51], v[80:83], a[144:147]\n"
      "ds_read_b64_tr_b16 v[68:69], v118 offset:51200\n"
      "v_mfma_f32_16x16x32_bf16 a[148:151], v[48:51], v[84:87], a[148:151]\n"
      "v_exp_f32 v5, v5\n"
      "v_mfma_f32_16x16x32_bf16 a[212:215], v[56:59], v[84:87], a[212:215]\n"
      "ds_read_b64_tr_b16 v[70:71], v118 offset:59392\n"
      "v_add_f32 v112, v112, v5\n"
      "v_mfma_f32_16x16x32_bf16 a[208:211], v[56:59], v[80:83], a[208:211]\n"
      "s_waitcnt lgkmcnt(4)\n"
      "v_cvt_pk_bf16_f32 v2, v4, v5\n"
      "v_mfma_f32_16x16x32_bf16 a[216:219], v[56:59], v[88:91], a[216:219]\n"
      "ds_read_b64_tr_b16 v[72:73], v117 offset:51712\n"
      "v_mfma_f32_16x16x32_bf16 a[220:223], v[56:59], v[92:95], a[220:223]\n"
      "v_exp_f32 v6, v6\n"
      "v_mfma_f32_16x16x32_bf16 a[156:159], v[48:51], v[92:95], a[156:159]\n"
      "ds_read_b64_tr_b16 v[74:75], v117 offset:59904\n"
      "v_add_f32 v112, v112, v6\n"
      "v_mfma_f32_16x16x32_bf16 a[152:155], v[48:51], v[88:91], a[152:155]\n"
      "v_mfma_f32_16x16x32_bf16 v[216:219], v[40:43], v[88:91], v[216:219]\n"
      "ds_read_b64_tr_b16 v[76:77], v118 offset:51712\n"
      "v_mfma_f32_16x16x32_bf16 v[220:223], v[40:43], v[92:95], v[220:223]\n"
      "v_exp_f32 v7, v7\n"
      "v_mfma_f32_16x16x32_bf16 v[156:159], v[32:35], v[92:95], v[156:159]\n"
      "ds_read_b64_tr_b16 v[78:79], v118 offset:59904\n"
      "v_add_f32 v112, v112, v7\n"
      "v_mfma_f32_16x16x32_bf16 v[152:155], v[32:35], v[88:91], v[152:155]\n"
      "s_waitcnt lgkmcnt(4)\n"
      "v_cvt_pk_bf16_f32 v3, v6, v7\n"
      "v_mfma_f32_16x16x32_bf16 v[160:163], v[32:35], v[64:67], v[160:163]\n"
      "ds_read_b64_tr_b16 v[80:81], v117 offset:52224\n"
      "v_mfma_f32_16x16x32_bf16 v[164:167], v[32:35], v[68:71], v[164:167]\n"
      "v_exp_f32 v8, v8\n"
      "v_mfma_f32_16x16x32_bf16 v[228:231], v[40:43], v[68:71], v[228:231]\n"
      "ds_read_b64_tr_b16 v[82:83], v117 offset:60416\n"
      "v_add_f32 v113, v113, v8\n"
      "v_mfma_f32_16x16x32_bf16 v[224:227], v[40:43], v[64:67], v[224:227]\n"
      "v_mfma_f32_16x16x32_bf16 a[160:163], v[48:51], v[64:67], a[160:163]\n"
      "ds_read_b64_tr_b16 v[84:85], v118 offset:52224\n"
      "v_mfma_f32_16x16x32_bf16 a[164:167], v[48:51], v[68:71], a[164:167]\n"
      "v_exp_f32 v9, v9\n"
      "v_mfma_f32_16x16x32_bf16 a[228:231], v[56:59], v[68:71], a[228:231]\n"
      "ds_read_b64_tr_b16 v[86:87], v118 offset:60416\n"
      "v_add_f32 v113, v113, v9\n"
      "v_mfma_f32_16x16x32_bf16 a[224:227], v[56:59], v[64:67], a[224:227]\n"
      "s_waitcnt lgkmcnt(4)\n"
      "v_cvt_pk_bf16_f32 v8, v8, v9\n"
      "v_mfma_f32_16x16x32_bf16 a[232:235], v[56:59], v[72:75], a[232:235]\n"
      "ds_read_b64_tr_b16 v[88:89], v117 offset:52736\n"
      "v_mfma_f32_16x16x32_bf16 a[236:239], v[56:59], v[76:79], a[236:239]\n"
      "v_exp_f32 v10, v10\n"
      "v_mfma_f32_16x16x32_bf16 a[172:175], v[48:51], v[76:79], a[172:175]\n"
      "ds_read_b64_tr_b16 v[90:91], v117 offset:60928\n"
      "v_add_f32 v113, v113, v10\n"
      "v_mfma_f32_16x16x32_bf16 a[168:171], v[48:51], v[72:75], a[168:171]\n"
      "v_mfma_f32_16x16x32_bf16 v[232:235], v[40:43], v[72:75], v[232:235]\n"
      "ds_read_b64_tr_b16 v[92:93], v118 offset:52736\n"
      "v_mfma_f32_16x16x32_bf16 v[236:239], v[40:43], v[76:79], v[236:239]\n"
      "v_exp_f32 v11, v11\n"
      "v_mfma_f32_16x16x32_bf16 v[172:175], v[32:35], v[76:79], v[172:175]\n"
      "ds_read_b64_tr_b16 v[94:95], v118 offset:60928\n"
      "v_add_f32 v113, v113, v11\n"
      "v_mfma_f32_16x16x32_bf16 v[168:171], v[32:35], v[72:75], v[168:171]\n"
      "s_waitcnt lgkmcnt(4)\n"
      "v_cvt_pk_bf16_f32 v9, v10, v11\n"
      "v_mfma_f32_16x16x32_bf16 v[176:179], v[32:35], v[80:83], v[176:179]\n"
      "ds_read_b128 v[64:67], v116 offset:16384\n"
      "v_mfma_f32_16x16x32_bf16 v[180:183], v[32:35], v[84:87], v[180:183]\n"
      "v_exp_f32 v12, v12\n"
      "v_mfma_f32_16x16x32_bf16 v[244:247], v[40:43], v[84:87], v[244:247]\n"
      "ds_read_b128 v[68:71], v116 offset:16896\n"
      "v_add_f32 v113, v113, v12\n"
      "v_mfma_f32_16x16x32_bf16 v[240:243], v[40:43], v[80:83], v[240:243]\n"
      "v_mfma_f32_16x16x32_bf16 a[176:179], v[48:51], v[80:83], a[176:179]\n"
      "v_mfma_f32_16x16x32_bf16 a[180:183], v[48:51], v[84:87], a[180:183]\n"
      "v_exp_f32 v13, v13\n"
      "v_mfma_f32_16x16x32_bf16 a[244:247], v[56:59], v[84:87], a[244:247]\n"
      "v_add_f32 v113, v113, v13\n"
      "v_cvt_pk_bf16_f32 v10, v12, v13\n"
      "v_mfma_f32_16x16x32_bf16 a[240:243], v[56:59], v[80:83], a[240:243]\n"
      "s_waitcnt lgkmcnt(2)\n"
      "v_mfma_f32_16x16x32_bf16 a[248:251], v[56:59], v[88:91], a[248:251]\n"
      "ds_read_b128 v[72:75], v116 offset:17408\n"
      "v_mfma_f32_16x16x32_bf16 a[252:255], v[56:59], v[92:95], a[252:255]\n"
      "v_exp_f32 v14, v14\n"
      "v_mfma_f32_16x16x32_bf16 a[188:191], v[48:51], v[92:95], a[188:191]\n"
      "ds_read_b128 v[76:79], v116 offset:17920\n"
      "v_add_f32 v113, v113, v14\n"
      "v_mfma_f32_16x16x32_bf16 a[184:187], v[48:51], v[88:91], a[184:187]\n"
      "v_mfma_f32_16x16x32_bf16 v[248:251], v[40:43], v[88:91], v[248:251]\n"
      "v_mfma_f32_16x16x32_bf16 v[252:255], v[40:43], v[92:95], v[252:255]\n"
      "v_exp_f32 v15, v15\n"
      "v_mfma_f32_16x16x32_bf16 v[188:191], v[32:35], v[92:95], v[188:191]\n"
      "v_add_f32 v113, v113, v15\n"
      "v_cvt_pk_bf16_f32 v11, v14, v15\n"
      "v_mfma_f32_16x16x32_bf16 v[184:187], v[32:35], v[88:91], v[184:187]\n"
      "s_waitcnt lgkmcnt(2)\n"
      "v_mfma_f32_16x16x32_bf16 v[32:35], v[64:67], a[0:3], v[96:99]\n"
      "ds_read_b128 v[80:83], v116 offset:18432\n"
      "s_mov_b32 s54, 0x5f800000\n"
      "v_mfma_f32_16x16x32_bf16 v[32:35], v[68:71], a[4:7], v[32:35]\n"
      "ds_read_b128 v[88:91], v116 offset:19456\n"
      "s_mov_b64 s[60:61], 0\n"
      "v_mfma_f32_16x16x32_bf16 v[40:43], v[68:71], a[36:39], v[100:103]\n"
      "ds_read_b128 v[84:87], v116 offset:18944\n"
      "s_add_u32 s55, s93, 98304\n"
      "v_mfma_f32_16x16x32_bf16 v[40:43], v[64:67], a[32:35], v[40:43]\n"
      "s_waitcnt lgkmcnt(3)\n"
      "ds_read_b128 v[92:95], v116 offset:19968\n"
      "v_mbcnt_lo_u32_b32 v52, -1, 0\n"
      "v_mfma_f32_16x16x32_bf16 v[40:43], v[72:75], a[40:43], v[40:43]\n"
      "ds_read_b128 v[64:67], v116 offset:24576\n"
      "v_mbcnt_hi_u32_b32 v52, -1, v52\n"
      "v_and_b32 v124, 48, v52\n"
      "v_mfma_f32_16x16x32_bf16 v[40:43], v[76:79], a[44:47], v[40:43]\n"
      "v_and_b32 v53, 15, v52\n"
      "v_lshlrev_b32 v125, 9, v53\n"
      "v_mfma_f32_16x16x32_bf16 v[32:35], v[76:79], a[12:15], v[32:35]\n"
      "ds_read_b128 v[68:71], v116 offset:25088\n"
      "v_bfe_u32 v54, v52, 3, 1\n"
      "v_mfma_f32_16x16x32_bf16 v[32:35], v[72:75], a[8:11], v[32:35]\n"
      "s_waitcnt lgkmcnt(3)\n"
      "v_lshlrev_b32 v54, 2, v54\n"
      "v_xor_b32 v53, v53, v54\n"
      "v_mfma_f32_16x16x32_bf16 v[32:35], v[80:83], a[16:19], v[32:35]\n"
      "ds_read_b128 v[72:75], v116 offset:25600\n"
      "v_lshlrev_b32 v119, 5, v53\n"
      "v_mfma_f32_16x16x32_bf16 v[32:35], v[84:87], a[20:23], v[32:35]\n"
      "v_lshrrev_b32 v54, 2, v52\n"
      "v_lshrrev_b32 v53, 4, v52\n"
      "v_xor_b32 v54, v54, v53\n"
      "v_mfma_f32_16x16x32_bf16 v[40:43], v[84:87], a[52:55], v[40:43]\n"
      "ds_read_b128 v[76:79], v116 offset:26112\n"
      "v_and_b32 v54, 3, v54\n"
      "v_mfma_f32_16x16x32_bf16 v[40:43], v[80:83], a[48:51], v[40:43]\n"
      "s_waitcnt lgkmcnt(4)\n"
      "v_lshl_add_u32 v119, v54, 3, v119\n"
      "v_add_u32 v119, s55, v119\n"
      "v_mfma_f32_16x16x32_bf16 v[40:43], v[88:91], a[56:59], v[40:43]\n"
      "ds_read_b128 v[80:83], v116 offset:26624\n"
      "v_add3_u32 v125, v125, v124, s49\n"
      "v_mfma_f32_16x16x32_bf16 v[40:43], v[92:95], a[60:63], v[40:43]\n"
      "v_lshlrev_b32 v120, 8, v53\n"
      "v_and_b32 v54, 1, v53\n"
      "v_mfma_f32_16x16x32_bf16 v[32:35], v[92:95], a[28:31], v[32:35]\n"
      "ds_read_b128 v[84:87], v116 offset:27136\n"
      "v_lshl_add_u32 v120, v54, 7, v120\n"
      "v_lshlrev_b32 v53, 1, v53\n"
      "v_mfma_f32_16x16x32_bf16 v[32:35], v[88:91], a[24:27], v[32:35]\n"
      "s_waitcnt lgkmcnt(4)\n"
      "v_xor_b32 v53, v53, v52\n"
      "v_and_b32 v53, 3, v53\n"
      "v_mfma_f32_16x16x32_bf16 v[36:39], v[64:67], a[0:3], v[96:99]\n"
      "ds_read_b128 v[88:91], v116 offset:27648\n"
      "v_lshl_add_u32 v120, v53, 3, v120\n"
      "v_mfma_f32_16x16x32_bf16 v[36:39], v[68:71], a[4:7], v[36:39]\n"
      "v_bfe_u32 v54, v52, 2, 2\n"
      "v_lshl_add_u32 v120, v54, 5, v120\n"
      "v_mfma_f32_16x16x32_bf16 v[44:47], v[68:71], a[36:39], v[100:103]\n"
      "ds_read_b128 v[92:95], v116 offset:28160\n"
      "v_add_u32 v120, s55, v120\n"
      "v_mfma_f32_16x16x32_bf16 v[44:47], v[64:67], a[32:35], v[44:47]\n"
      "s_waitcnt lgkmcnt(4)\n"
      "v_xor_b32 v121, 0x88, v120\n"
      "v_add_u32 v122, 0x10000, v117\n"
      "v_add_u32 v123, 0x10000, v118\n"
      "v_mfma_f32_16x16x32_bf16 v[44:47], v[72:75], a[40:43], v[44:47]\n"
      "ds_read_b128 v[64:67], v116 offset:0\n"
      "v_mfma_f32_16x16x32_bf16 v[44:47], v[76:79], a[44:47], v[44:47]\n"
      "v_exp_f32 v32, v32\n"
      "v_mfma_f32_16x16x32_bf16 v[36:39], v[76:79], a[12:15], v[36:39]\n"
      "ds_read_b128 v[68:71], v116 offset:512\n"
      "v_add_f32 v112, v112, v32\n"
      "v_mfma_f32_16x16x32_bf16 v[36:39], v[72:75], a[8:11], v[36:39]\n"
      "s_waitcnt lgkmcnt(4)\n"
      "v_exp_f32 v33, v33\n"
      "v_mfma_f32_16x16x32_bf16 v[36:39], v[80:83], a[16:19], v[36:39]\n"
      "ds_read_b128 v[72:75], v116 offset:1024\n"
      "v_add_f32 v112, v112, v33\n"
      "v_mfma_f32_16x16x32_bf16 v[36:39], v[84:87], a[20:23], v[36:39]\n"
      "v_exp_f32 v34, v34\n"
      "v_mfma_f32_16x16x32_bf16 v[44:47], v[84:87], a[52:55], v[44:47]\n"
      "ds_read_b128 v[76:79], v116 offset:1536\n"
      "v_cvt_pk_bf16_f32 v32, v32, v33\n"
      "v_mfma_f32_16x16x32_bf16 v[44:47], v[80:83], a[48:51], v[44:47]\n"
      "s_waitcnt lgkmcnt(4)\n"
      "v_exp_f32 v35, v35\n"
      "v_mfma_f32_16x16x32_bf16 v[44:47], v[88:91], a[56:59], v[44:47]\n"
      "ds_read_b128 v[80:83], v116 offset:2048\n"
      "v_add_f32 v112, v112, v34\n"
      "v_mfma_f32_16x16x32_bf16 v[44:47], v[92:95], a[60:63], v[44:47]\n"
      "v_exp_f32 v40, v40\n"
      "v_mfma_f32_16x16x32_bf16 v[36:39], v[92:95], a[28:31], v[36:39]\n"
      "ds_read_b128 v[84:87], v116 offset:2560\n"
      "v_add_f32 v112, v112, v35\n"
      "v_mfma_f32_16x16x32_bf16 v[36:39], v[88:91], a[24:27], v[36:39]\n"
      "s_waitcnt lgkmcnt(4)\n"
      "v_exp_f32 v41, v41\n"
      "v_mfma_f32_16x16x32_bf16 v[16:19], v[64:67], a[64:67], v[104:107]\n"
      "ds_read_b128 v[88:91], v116 offset:3072\n"
      "v_cvt_pk_bf16_f32 v33, v34, v35\n"
      "v_mfma_f32_16x16x32_bf16 v[16:19], v[68:71], a[68:71], v[16:19]\n"
      "v_exp_f32 v42, v42\n"
      "v_mfma_f32_16x16x32_bf16 v[24:27], v[68:71], a[100:103], v[108:111]\n"
      "ds_read_b128 v[92:95], v116 offset:3584\n"
      "v_add_f32 v113, v113, v40\n"
      "v_mfma_f32_16x16x32_bf16 v[24:27], v[64:67], a[96:99], v[24:27]\n"
      "s_waitcnt lgkmcnt(4)\n"
      "v_exp_f32 v43, v43\n"
      "v_mfma_f32_16x16x32_bf16 v[24:27], v[72:75], a[104:107], v[24:27]\n"
      "ds_read_b128 v[64:67], v116 offset:8192\n"
      "v_add_f32 v113, v113, v41\n"
      "v_mfma_f32_16x16x32_bf16 v[24:27], v[76:79], a[108:111], v[24:27]\n"
      "v_exp_f32 v36, v36\n"
      "v_mfma_f32_16x16x32_bf16 v[16:19], v[76:79], a[76:79], v[16:19]\n"
      "ds_read_b128 v[68:71], v116 offset:8704\n"
      "v_cvt_pk_bf16_f32 v40, v40, v41\n"
      "v_mfma_f32_16x16x32_bf16 v[16:19], v[72:75], a[72:75], v[16:19]\n"
      "s_waitcnt lgkmcnt(4)\n"
      "v_exp_f32 v37, v37\n"
      "v_mfma_f32_16x16x32_bf16 v[16:19], v[80:83], a[80:83], v[16:19]\n"
      "ds_read_b128 v[72:75], v116 offset:9216\n"
      "v_add_f32 v113, v113, v42\n"
      "v_mfma_f32_16x16x32_bf16 v[16:19], v[84:87], a[84:87], v[16:19]\n"
      "v_exp_f32 v38, v38\n"
      "v_mfma_f32_16x16x32_bf16 v[24:27], v[84:87], a[116:119], v[24:27]\n"
      "ds_read_b128 v[76:79], v116 offset:9728\n"
      "v_add_f32 v113, v113, v43\n"
      "v_mfma_f32_16x16x32_bf16 v[24:27], v[80:83], a[112:115], v[24:27]\n"
      "s_waitcnt lgkmcnt(4)\n"
      "v_exp_f32 v39, v39\n"
      "v_mfma_f32_16x16x32_bf16 v[24:27], v[88:91], a[120:123], v[24:27]\n"
      "ds_read_b128 v[80:83], v116 offset:10240\n"
      "v_cvt_pk_bf16_f32 v41, v42, v43\n"
      "v_add_f32 v112, v112, v36\n"
      "v_mfma_f32_16x16x32_bf16 v[24:27], v[92:95], a[124:127], v[24:27]\n"
      "s_waitcnt vmcnt(12)\n"
      "s_barrier\n"
      "v_exp_f32 v44, v44\n"
      "v_mfma_f32_16x16x32_bf16 v[16:19], v[92:95], a[92:95], v[16:19]\n"
      "ds_read_b128 v[84:87], v116 offset:10752\n"
      "v_add_f32 v112, v112, v37\n"
      "v_mfma_f32_16x16x32_bf16 v[16:19], v[88:91], a[88:91], v[16:19]\n"
      "s_waitcnt lgkmcnt(4)\n"
      "v_exp_f32 v45, v45\n"
      "v_mfma_f32_16x16x32_bf16 v[20:23], v[64:67], a[64:67], v[104:107]\n"
      "ds_read_b128 v[88:91], v116 offset:11264\n"
      "v_cvt_pk_bf16_f32 v34, v36, v37\n"
      "v_add_f32 v112, v112, v38\n"
      "v_mfma_f32_16x16x32_bf16 v[20:23], v[68:71], a[68:71], v[20:23]\n"
      "v_exp_f32 v46, v46\n"
      "v_mfma_f32_16x16x32_bf16 v[28:31], v[68:71], a[100:103], v[108:111]\n"
      "ds_read_b128 v[92:95], v116 offset:11776\n"
      "v_add_f32 v112, v112, v39\n"
      "v_mfma_f32_16x16x32_bf16 v[28:31], v[64:67], a[96:99], v[28:31]\n"
      "s_waitcnt lgkmcnt(4)\n"
      "v_exp_f32 v47, v47\n"
      "v_mfma_f32_16x16x32_bf16 v[28:31], v[72:75], a[104:107], v[28:31]\n"
      "ds_read_b128 v[64:67], v116 offset:32768\n"
      "v_cvt_pk_bf16_f32 v35, v38, v39\n"
      "v_add_f32 v113, v113, v44\n"
      "v_mfma_f32_16x16x32_bf16 v[28:31], v[76:79], a[108:111], v[28:31]\n"
      "v_exp_f32 v16, v16\n"
      "v_mfma_f32_16x16x32_bf16 v[20:23], v[76:79], a[76:79], v[20:23]\n"
      "ds_read_b128 v[68:71], v116 offset:33280\n"
      "v_add_f32 v113, v113, v45\n"
      "v_mfma_f32_16x16x32_bf16 v[20:23], v[72:75], a[72:75], v[20:23]\n"
      "s_waitcnt lgkmcnt(4)\n"
      "v_exp_f32 v17, v17\n"
      "v_mfma_f32_16x16x32_bf16 v[20:23], v[80:83], a[80:83], v[20:23]\n"
      "ds_read_b128 v[72:75], v116 offset:33792\n"
      "v_cvt_pk_bf16_f32 v42, v44, v45\n"
      "v_add_f32 v113, v113, v46\n"
      "v_mfma_f32_16x16x32_bf16 v[20:23], v[84:87], a[84:87], v[20:23]\n"
      "v_exp_f32 v18, v18\n"
      "v_mfma_f32_16x16x32_bf16 v[28:31], v[84:87], a[116:119], v[28:31]\n"
      "ds_read_b128 v[76:79], v116 offset:34304\n"
      "v_add_f32 v113, v113, v47\n"
      "v_mfma_f32_16x16x32_bf16 v[28:31], v[80:83], a[112:115], v[28:31]\n"
      "s_waitcnt lgkmcnt(4)\n"
      "v_exp_f32 v19, v19\n"
      "v_mfma_f32_16x16x32_bf16 v[28:31], v[88:91], a[120:123], v[28:31]\n"
      "ds_read_b128 v[80:83], v116 offset:34816\n"
      "v_cvt_pk_bf16_f32 v43, v46, v47\n"
      "v_add_f32 v114, v114, v16\n"
      "v_mfma_f32_16x16x32_bf16 v[28:31], v[92:95], a[124:127], v[28:31]\n"
      "v_exp_f32 v24, v24\n"
      "v_mfma_f32_16x16x32_bf16 v[20:23], v[92:95], a[92:95], v[20:23]\n"
      "ds_read_b128 v[84:87], v116 offset:35328\n"
      "v_add_f32 v114, v114, v17\n"
      "v_mfma_f32_16x16x32_bf16 v[20:23], v[88:91], a[88:91], v[20:23]\n"
      "s_waitcnt lgkmcnt(4)\n"
      "v_exp_f32 v25, v25\n"
      "v_mfma_f32_16x16x32_bf16 v[48:51], v[64:67], a[0:3], v[96:99]\n"
      "ds_read_b128 v[88:91], v116 offset:35840\n"
      "v_cvt_pk_bf16_f32 v16, v16, v17\n"
      "v_add_f32 v114, v114, v18\n"
      "v_mfma_f32_16x16x32_bf16 v[48:51], v[68:71], a[4:7], v[48:51]\n"
      "v_exp_f32 v26, v26\n"
      "v_mfma_f32_16x16x32_bf16 v[56:59], v[68:71], a[36:39], v[100:103]\n"
      "ds_read_b128 v[92:95], v116 offset:36352\n"
      "v_add_f32 v114, v114, v19\n"
      "v_mfma_f32_16x16x32_bf16 v[56:59], v[64:67], a[32:35], v[56:59]\n"
      "s_waitcnt lgkmcnt(4)\n"
      "v_exp_f32 v27, v27\n"
      "v_mfma_f32_16x16x32_bf16 v[56:59], v[72:75], a[40:43], v[56:59]\n"
      "ds_read_b128 v[64:67], v116 offset:40960\n"
      "v_cvt_pk_bf16_f32 v17, v18, v19\n"
      "v_add_f32 v115, v115, v24\n"
      "v_mfma_f32_16x16x32_bf16 v[56:59], v[76:79], a[44:47], v[56:59]\n"
      "v_exp_f32 v20, v20\n"
      "v_mfma_f32_16x16x32_bf16 v[48:51], v[76:79], a[12:15], v[48:51]\n"
      "ds_read_b128 v[68:71], v116 offset:41472\n"
      "v_add_f32 v115, v115, v25\n"
      "v_mfma_f32_16x16x32_bf16 v[48:51], v[72:75], a[8:11], v[48:51]\n"
      "s_waitcnt lgkmcnt(4)\n"
      "v_exp_f32 v21, v21\n"
      "v_mfma_f32_16x16x32_bf16 v[48:51], v[80:83], a[16:19], v[48:51]\n"
      "ds_read_b128 v[72:75], v116 offset:41984\n"
      "v_cvt_pk_bf16_f32 v24, v24, v25\n"
      "v_add_f32 v115, v115, v26\n"
      "v_mfma_f32_16x16x32_bf16 v[48:51], v[84:87], a[20:23], v[48:51]\n"
      "v_exp_f32 v22, v22\n"
      "v_mfma_f32_16x16x32_bf16 v[56:59], v[84:87], a[52:55], v[56:59]\n"
      "ds_read_b128 v[76:79], v116 offset:42496\n"
      "v_add_f32 v115, v115, v27\n"
      "v_mfma_f32_16x16x32_bf16 v[56:59], v[80:83], a[48:51], v[56:59]\n"
      "s_waitcnt lgkmcnt(4)\n"
      "v_exp_f32 v23, v23\n"
      "v_mfma_f32_16x16x32_bf16 v[56:59], v[88:91], a[56:59], v[56:59]\n"
      "ds_read_b128 v[80:83], v116 offset:43008\n"
      "v_cvt_pk_bf16_f32 v25, v26, v27\n"
      "v_add_f32 v114, v114, v20\n"
      "v_mfma_f32_16x16x32_bf16 v[56:59], v[92:95], a[60:63], v[56:59]\n"
      "s_waitcnt vmcnt(5)\n"
      "s_barrier\n"
      "v_exp_f32 v28, v28\n"
      "v_mfma_f32_16x16x32_bf16 v[48:51], v[92:95], a[28:31], v[48:51]\n"
      "ds_read_b128 v[84:87], v116 offset:43520\n"
      "v_add_f32 v114, v114, v21\n"
      "v_mfma_f32_16x16x32_bf16 v[48:51], v[88:91], a[24:27], v[48:51]\n"
      "s_waitcnt lgkmcnt(4)\n"
      "v_exp_f32 v29, v29\n"
      "v_mfma_f32_16x16x32_bf16 v[52:55], v[64:67], a[0:3], v[96:99]\n"
      "ds_read_b128 v[88:91], v116 offset:44032\n"
      "v_cvt_pk_bf16_f32 v18, v20, v21\n"
      "v_add_f32 v114, v114, v22\n"
      "v_mfma_f32_16x16x32_bf16 v[52:55], v[68:71], a[4:7], v[52:55]\n"
      "v_exp_f32 v30, v30\n"
      "v_mfma_f32_16x16x32_bf16 v[60:63], v[68:71], a[36:39], v[100:103]\n"
      "ds_read_b128 v[92:95], v116 offset:44544\n"
      "v_add_f32 v114, v114, v23\n"
      "v_mfma_f32_16x16x32_bf16 v[60:63], v[64:67], a[32:35], v[60:63]\n"
      "s_waitcnt lgkmcnt(4)\n"
      "v_exp_f32 v31, v31\n"
      "v_mfma_f32_16x16x32_bf16 v[60:63], v[72:75], a[40:43], v[60:63]\n"
      "ds_read_b128 v[64:67], v116 offset:49152\n"
      "v_cvt_pk_bf16_f32 v19, v22, v23\n"
      "v_add_f32 v115, v115, v28\n"
      "v_mfma_f32_16x16x32_bf16 v[60:63], v[76:79], a[44:47], v[60:63]\n"
      "v_exp_f32 v48, v48\n"
      "v_mfma_f32_16x16x32_bf16 v[52:55], v[76:79], a[12:15], v[52:55]\n"
      "ds_read_b128 v[68:71], v116 offset:49664\n"
      "v_add_f32 v115, v115, v29\n"
      "v_mfma_f32_16x16x32_bf16 v[52:55], v[72:75], a[8:11], v[52:55]\n"
      "s_waitcnt lgkmcnt(4)\n"
      "v_exp_f32 v49, v49\n"
      "v_mfma_f32_16x16x32_bf16 v[52:55], v[80:83], a[16:19], v[52:55]\n"
      "ds_read_b128 v[72:75], v116 offset:50176\n"
      "v_cvt_pk_bf16_f32 v26, v28, v29\n"
      "v_add_f32 v115, v115, v30\n"
      "v_mfma_f32_16x16x32_bf16 v[52:55], v[84:87], a[20:23], v[52:55]\n"
      "v_exp_f32 v50, v50\n"
      "v_mfma_f32_16x16x32_bf16 v[60:63], v[84:87], a[52:55], v[60:63]\n"
      "ds_read_b128 v[76:79], v116 offset:50688\n"
      "v_add_f32 v115, v115, v31\n"
      "v_mfma_f32_16x16x32_bf16 v[60:63], v[80:83], a[48:51], v[60:63]\n"
      "s_waitcnt lgkmcnt(4)\n"
      "v_exp_f32 v51, v51\n"
      "v_mfma_f32_16x16x32_bf16 v[60:63], v[88:91], a[56:59], v[60:63]\n"
      "ds_read_b128 v[80:83], v116 offset:51200\n"
      "v_cvt_pk_bf16_f32 v27, v30, v31\n"
      "v_add_f32 v112, v112, v48\n"
      "v_mfma_f32_16x16x32_bf16 v[60:63], v[92:95], a[60:63], v[60:63]\n"
      "v_exp_f32 v56, v56\n"
      "v_mfma_f32_16x16x32_bf16 v[52:55], v[92:95], a[28:31], v[52:55]\n"
      "ds_read_b128 v[84:87], v116 offset:51712\n"
      "v_add_f32 v112, v112, v49\n"
      "v_mfma_f32_16x16x32_bf16 v[52:55], v[88:91], a[24:27], v[52:55]\n"
      "s_waitcnt lgkmcnt(4)\n"
      "v_exp_f32 v57, v57\n"
      "v_mfma_f32_16x16x32_bf16 v[4:7], v[64:67], a[0:3], v[96:99]\n"
      "ds_read_b128 v[88:91], v116 offset:52224\n"
      "v_cvt_pk_bf16_f32 v48, v48, v49\n"
      "v_add_f32 v112, v112, v50\n"
      "v_mfma_f32_16x16x32_bf16 v[4:7], v[68:71], a[4:7], v[4:7]\n"
      "v_exp_f32 v58, v58\n"
      "v_mfma_f32_16x16x32_bf16 v[36:39], v[68:71], a[36:39], v[100:103]\n"
      "ds_read_b128 v[92:95], v116 offset:52736\n"
      "v_add_f32 v112, v112, v51\n"
      "v_mfma_f32_16x16x32_bf16 v[36:39], v[64:67], a[32:35], v[36:39]\n"
      "s_waitcnt lgkmcnt(4)\n"
      "v_exp_f32 v59, v59\n"
      "v_mfma_f32_16x16x32_bf16 v[36:39], v[72:75], a[40:43], v[36:39]\n"
      "ds_read_b128 v[64:67], v116 offset:57344\n"
      "v_cvt_pk_bf16_f32 v49, v50, v51\n"
      "v_add_f32 v113, v113, v56\n"
      "v_mfma_f32_16x16x32_bf16 v[36:39], v[76:79], a[44:47], v[36:39]\n"
      "v_exp_f32 v52, v52\n"
      "v_mfma_f32_16x16x32_bf16 v[4:7], v[76:79], a[12:15], v[4:7]\n"
      "ds_read_b128 v[68:71], v116 offset:57856\n"
      "v_add_f32 v113, v113, v57\n"
      "v_mfma_f32_16x16x32_bf16 v[4:7], v[72:75], a[8:11], v[4:7]\n"
      "s_waitcnt lgkmcnt(4)\n"
      "v_exp_f32 v53, v53\n"
      "v_mfma_f32_16x16x32_bf16 v[4:7], v[80:83], a[16:19], v[4:7]\n"
      "ds_read_b128 v[72:75], v116 offset:58368\n"
      "v_cvt_pk_bf16_f32 v56, v56, v57\n"
      "v_add_f32 v113, v113, v58\n"
      "v_mfma_f32_16x16x32_bf16 v[4:7], v[84:87], a[20:23], v[4:7]\n"
      "v_exp_f32 v54, v54\n"
      "v_mfma_f32_16x16x32_bf16 v[36:39], v[84:87], a[52:55], v[36:39]\n"
      "ds_read_b128 v[76:79], v116 offset:58880\n"
      "v_add_f32 v113, v113, v59\n"
      "v_mfma_f32_16x16x32_bf16 v[36:39], v[80:83], a[48:51], v[36:39]\n"
      "s_waitcnt lgkmcnt(4)\n"
      "v_exp_f32 v55, v55\n"
      "v_mfma_f32_16x16x32_bf16 v[36:39], v[88:91], a[56:59], v[36:39]\n"
      "ds_read_b128 v[80:83], v116 offset:59392\n"
      "v_cvt_pk_bf16_f32 v57, v58, v59\n"
      "v_add_f32 v112, v112, v52\n"
      "v_mfma_f32_16x16x32_bf16 v[36:39], v[92:95], a[60:63], v[36:39]\n"
      "v_exp_f32 v60, v60\n"
      "v_mfma_f32_16x16x32_bf16 v[4:7], v[92:95], a[28:31], v[4:7]\n"
      "ds_read_b128 v[84:87], v116 offset:59904\n"
      "v_add_f32 v112, v112, v53\n"
      "v_mfma_f32_16x16x32_bf16 v[4:7], v[88:91], a[24:27], v[4:7]\n"
      "s_waitcnt lgkmcnt(4)\n"
      "v_exp_f32 v61, v61\n"
      "v_mfma_f32_16x16x32_bf16 v[12:15], v[64:67], a[0:3], v[96:99]\n"
      "ds_read_b128 v[88:91], v116 offset:60416\n"
      "v_cvt_pk_bf16_f32 v50, v52, v53\n"
      "v_add_f32 v112, v112, v54\n"
      "v_mfma_f32_16x16x32_bf16 v[12:15], v[68:71], a[4:7], v[12:15]\n"
      "v_exp_f32 v62, v62\n"
      "v_mfma_f32_16x16x32_bf16 v[44:47], v[68:71], a[36:39], v[100:103]\n"
      "ds_read_b128 v[92:95], v116 offset:60928\n"
      "v_add_f32 v112, v112, v55\n"
      "v_mfma_f32_16x16x32_bf16 v[44:47], v[64:67], a[32:35], v[44:47]\n"
      "s_waitcnt lgkmcnt(4)\n"
      "v_exp_f32 v63, v63\n"
      "v_mfma_f32_16x16x32_bf16 v[44:47], v[72:75], a[40:43], v[44:47]\n"
      "ds_read_b128 v[64:67], v116 offset:16384\n"
      "v_cvt_pk_bf16_f32 v51, v54, v55\n"
      "v_add_f32 v113, v113, v60\n"
      "v_mfma_f32_16x16x32_bf16 v[44:47], v[76:79], a[44:47], v[44:47]\n"
      "v_exp_f32 v4, v4\n"
      "v_mfma_f32_16x16x32_bf16 v[12:15], v[76:79], a[12:15], v[12:15]\n"
      "ds_read_b128 v[68:71], v116 offset:16896\n"
      "v_add_f32 v113, v113, v61\n"
      "v_mfma_f32_16x16x32_bf16 v[12:15], v[72:75], a[8:11], v[12:15]\n"
      "s_waitcnt lgkmcnt(4)\n"
      "v_exp_f32 v5, v5\n"
      "v_mfma_f32_16x16x32_bf16 v[12:15], v[80:83], a[16:19], v[12:15]\n"
      "ds_read_b128 v[72:75], v116 offset:17408\n"
      "v_cvt_pk_bf16_f32 v58, v60, v61\n"
      "v_add_f32 v113, v113, v62\n"
      "v_mfma_f32_16x16x32_bf16 v[12:15], v[84:87], a[20:23], v[12:15]\n"
      "v_exp_f32 v6, v6\n"
      "v_mfma_f32_16x16x32_bf16 v[44:47], v[84:87], a[52:55], v[44:47]\n"
      "ds_read_b128 v[76:79], v116 offset:17920\n"
      "v_add_f32 v113, v113, v63\n"
      "v_mfma_f32_16x16x32_bf16 v[44:47], v[80:83], a[48:51], v[44:47]\n"
      "s_waitcnt lgkmcnt(4)\n"
      "v_exp_f32 v7, v7\n"
      "v_mfma_f32_16x16x32_bf16 v[44:47], v[88:91], a[56:59], v[44:47]\n"
      "ds_read_b128 v[80:83], v116 offset:18432\n"
      "v_cvt_pk_bf16_f32 v59, v62, v63\n"
      "v_add_f32 v112, v112, v4\n"
      "v_mfma_f32_16x16x32_bf16 v[44:47], v[92:95], a[60:63], v[44:47]\n"
      "v_exp_f32 v36, v36\n"
      "v_mfma_f32_16x16x32_bf16 v[12:15], v[92:95], a[28:31], v[12:15]\n"
      "ds_read_b128 v[84:87], v116 offset:18944\n"
      "v_add_f32 v112, v112, v5\n"
      "v_mfma_f32_16x16x32_bf16 v[12:15], v[88:91], a[24:27], v[12:15]\n"
      "s_waitcnt lgkmcnt(4)\n"
      "v_exp_f32 v37, v37\n"
      "v_mfma_f32_16x16x32_bf16 v[20:23], v[64:67], a[64:67], v[104:107]\n"
      "ds_read_b128 v[88:91], v116 offset:19456\n"
      "v_cvt_pk_bf16_f32 v4, v4, v5\n"
      "v_add_f32 v112, v112, v6\n"
      "v_mfma_f32_16x16x32_bf16 v[20:23], v[68:71], a[68:71], v[20:23]\n"
      "v_exp_f32 v38, v38\n"
      "v_mfma_f32_16x16x32_bf16 v[28:31], v[68:71], a[100:103], v[108:111]\n"
      "ds_read_b128 v[92:95], v116 offset:19968\n"
      "v_add_f32 v112, v112, v7\n"
      "v_mfma_f32_16x16x32_bf16 v[28:31], v[64:67], a[96:99], v[28:31]\n"
      "s_waitcnt lgkmcnt(4)\n"
      "v_exp_f32 v39, v39\n"
      "v_mfma_f32_16x16x32_bf16 v[28:31], v[72:75], a[104:107], v[28:31]\n"
      "ds_read_b128 v[64:67], v116 offset:24576\n"
      "v_cvt_pk_bf16_f32 v5, v6, v7\n"
      "v_add_f32 v113, v113, v36\n"
      "v_mfma_f32_16x16x32_bf16 v[28:31], v[76:79], a[108:111], v[28:31]\n"
      "v_exp_f32 v12, v12\n"
      "v_mfma_f32_16x16x32_bf16 v[20:23], v[76:79], a[76:79], v[20:23]\n"
      "ds_read_b128 v[68:71], v116 offset:25088\n"
      "v_add_f32 v113, v113, v37\n"
      "v_mfma_f32_16x16x32_bf16 v[20:23], v[72:75], a[72:75], v[20:23]\n"
      "s_waitcnt lgkmcnt(4)\n"
      "v_exp_f32 v13, v13\n"
      "v_mfma_f32_16x16x32_bf16 v[20:23], v[80:83], a[80:83], v[20:23]\n"
      "ds_read_b128 v[72:75], v116 offset:25600\n"
      "v_cvt_pk_bf16_f32 v36, v36, v37\n"
      "v_add_f32 v113, v113, v38\n"
      "v_mfma_f32_16x16x32_bf16 v[20:23], v[84:87], a[84:87], v[20:23]\n"
      "v_exp_f32 v14, v14\n"
      "v_mfma_f32_16x16x32_bf16 v[28:31], v[84:87], a[116:119], v[28:31]\n"
      "ds_read_b128 v[76:79], v116 offset:26112\n"
      "v_add_f32 v113, v113, v39\n"
      "v_mfma_f32_16x16x32_bf16 v[28:31], v[80:83], a[112:115], v[28:31]\n"
      "s_waitcnt lgkmcnt(4)\n"
      "v_exp_f32 v15, v15\n"
      "v_mfma_f32_16x16x32_bf16 v[28:31], v[88:91], a[120:123], v[28:31]\n"
      "ds_read_b128 v[80:83], v116 offset:26624\n"
      "v_cvt_pk_bf16_f32 v37, v38, v39\n"
      "v_add_f32 v112, v112, v12\n"
      "v_mfma_f32_16x16x32_bf16 v[28:31], v[92:95], a[124:127], v[28:31]\n"
      "s_waitcnt vmcnt(0)\n"
      "s_barrier\n"
      "v_exp_f32 v44, v44\n"
      "v_mfma_f32_16x16x32_bf16 v[20:23], v[92:95], a[92:95], v[20:23]\n"
      "ds_read_b128 v[84:87], v116 offset:27136\n"
      "v_add_f32 v112, v112, v13\n"
      "v_mfma_f32_16x16x32_bf16 v[20:23], v[88:91], a[88:91], v[20:23]\n"
      "s_waitcnt lgkmcnt(4)\n"
      "v_exp_f32 v45, v45\n"
      "v_mfma_f32_16x16x32_bf16 v[52:55], v[64:67], a[64:67], v[104:107]\n"
      "ds_read_b128 v[88:91], v116 offset:27648\n"
      "v_cvt_pk_bf16_f32 v6, v12, v13\n"
      "v_add_f32 v112, v112, v14\n"
      "v_mfma_f32_16x16x32_bf16 v[52:55], v[68:71], a[68:71], v[52:55]\n"
      "v_exp_f32 v46, v46\n"
      "v_mfma_f32_16x16x32_bf16 v[60:63], v[68:71], a[100:103], v[108:111]\n"
      "ds_read_b128 v[92:95], v116 offset:28160\n"
      "v_add_f32 v112, v112, v15\n"
      "v_mfma_f32_16x16x32_bf16 v[60:63], v[64:67], a[96:99], v[60:63]\n"
      "s_waitcnt lgkmcnt(4)\n"
      "v_exp_f32 v47, v47\n"
      "v_mfma_f32_16x16x32_bf16 v[60:63], v[72:75], a[104:107], v[60:63]\n"
      "ds_read_b64_tr_b16 v[64:65], v117 offset:0\n"
      "v_cvt_pk_bf16_f32 v7, v14, v15\n"
      "v_add_f32 v113, v113, v44\n"
      "v_mfma_f32_16x16x32_bf16 v[60:63], v[76:79], a[108:111], v[60:63]\n"
      "ds_read_b64_tr_b16 v[68:69], v117 offset:16384\n"
      "v_add_f32 v113, v113, v45\n"
      "v_mfma_f32_16x16x32_bf16 v[52:55], v[76:79], a[76:79], v[52:55]\n"
      "ds_read_b64_tr_b16 v[66:67], v117 offset:8192\n"
      "v_cvt_pk_bf16_f32 v38, v44, v45\n"
      "v_mfma_f32_16x16x32_bf16 v[52:55], v[72:75], a[72:75], v[52:55]\n"
      "s_waitcnt lgkmcnt(5)\n"
      "ds_read_b64_tr_b16 v[70:71], v117 offset:24576\n"
      "v_add_f32 v113, v113, v46\n"
      "v_mfma_f32_16x16x32_bf16 v[52:55], v[80:83], a[80:83], v[52:55]\n"
      "ds_read_b64_tr_b16 v[72:73], v122 offset:0\n"
      "v_add_f32 v113, v113, v47\n"
      "v_mfma_f32_16x16x32_bf16 v[52:55], v[84:87], a[84:87], v[52:55]\n"
      "ds_read_b64_tr_b16 v[76:77], v122 offset:16384\n"
      "v_cvt_pk_bf16_f32 v39, v46, v47\n"
      "v_mfma_f32_16x16x32_bf16 v[60:63], v[84:87], a[116:119], v[60:63]\n"
      "ds_read_b64_tr_b16 v[74:75], v122 offset:8192\n"
      "v_mfma_f32_16x16x32_bf16 v[60:63], v[80:83], a[112:115], v[60:63]\n"
      "s_waitcnt lgkmcnt(7)\n"
      "ds_read_b64_tr_b16 v[78:79], v122 offset:24576\n"
      "v_add_u32 v96, 8192, v125\n"
      "v_mfma_f32_16x16x32_bf16 v[60:63], v[88:91], a[120:123], v[60:63]\n"
      "ds_read_b64_tr_b16 v[80:81], v123 offset:0\n"
      "v_mfma_f32_16x16x32_bf16 v[60:63], v[92:95], a[124:127], v[60:63]\n"
      "ds_read_b64_tr_b16 v[84:85], v123 offset:16384\n"
      "v_add_u32 v97, 16384, v125\n"
      "v_mfma_f32_16x16x32_bf16 v[52:55], v[92:95], a[92:95], v[52:55]\n"
      "ds_read_b64_tr_b16 v[82:83], v123 offset:8192\n"
      "v_mfma_f32_16x16x32_bf16 v[52:55], v[88:91], a[88:91], v[52:55]\n"
      "s_waitcnt lgkmcnt(7)\n"
      "ds_read_b64_tr_b16 v[86:87], v123 offset:24576\n"
      "v_add_u32 v98, 24576, v125\n"
      "v_mfma_f32_16x16x32_bf16 v[128:131], v[0:3], v[64:67], v[128:131]\n"
      "ds_read_b64_tr_b16 v[88:89], v118 offset:0\n"
      "v_mov_b32 v100, v112\n"
      "v_mfma_f32_16x16x32_bf16 v[128:131], v[32:35], v[68:71], v[128:131]\n"
      "ds_read_b64_tr_b16 v[92:93], v118 offset:16384\n"
      "v_mov_b32 v101, v113\n"
      "s_nop 0\n"
      "v_mfma_f32_16x16x32_bf16 v[192:195], v[40:43], v[68:71], v[192:195]\n"
      "ds_read_b64_tr_b16 v[90:91], v118 offset:8192\n"
      "v_permlane32_swap_b32 v112, v100\n"
      "v_permlane32_swap_b32 v113, v101\n"
      "v_mfma_f32_16x16x32_bf16 v[192:195], v[8:11], v[64:67], v[192:195]\n"
      "s_waitcnt lgkmcnt(7)\n"
      "ds_read_b64_tr_b16 v[94:95], v118 offset:24576\n"
      "v_add_f32 v112, v112, v100\n"
      "v_add_f32 v113, v113, v101\n"
      "v_mfma_f32_16x16x32_bf16 v[192:195], v[56:59], v[72:75], v[192:195]\n"
      "ds_read_b64_tr_b16 v[64:65], v117 offset:512\n"
      "v_mov_b32 v100, v112\n"
      "v_mfma_f32_16x16x32_bf16 v[192:195], v[36:39], v[76:79], v[192:195]\n"
      "ds_read_b64_tr_b16 v[68:69], v117 offset:16896\n"
      "v_mov_b32 v101, v113\n"
      "s_nop 0\n"
      "v_mfma_f32_16x16x32_bf16 v[128:131], v[4:7], v[76:79], v[128:131]\n"
      "ds_read_b64_tr_b16 v[66:67], v117 offset:8704\n"
      "v_permlane16_swap_b32 v112, v100\n"
      "v_permlane16_swap_b32 v113, v101\n"
      "v_mfma_f32_16x16x32_bf16 v[128:131], v[48:51], v[72:75], v[128:131]\n"
      "s_waitcnt lgkmcnt(7)\n"
      "ds_read_b64_tr_b16 v[70:71], v117 offset:25088\n"
      "v_add_f32 v112, v112, v100\n"
      "v_add_f32 v113, v113, v101\n"
      "v_mfma_f32_16x16x32_bf16 v[132:135], v[48:51], v[80:83], v[132:135]\n"
      "ds_read_b64_tr_b16 v[72:73], v122 offset:512\n"
      "v_rcp_f32 v102, v112\n"
      "v_mfma_f32_16x16x32_bf16 v[132:135], v[4:7], v[84:87], v[132:135]\n"
      "ds_read_b64_tr_b16 v[76:77], v122 offset:16896\n"
      "v_rcp_f32 v103, v113\n"
      "v_cmp_nge_f32 vcc, s54, v112\n"
      "v_mfma_f32_16x16x32_bf16 v[196:199], v[36:39], v[84:87], v[196:199]\n"
      "ds_read_b64_tr_b16 v[74:75], v122 offset:8704\n"
      "s_or_b64 s[60:61], s[60:61], vcc\n"
      "v_cmp_nge_f32 vcc, s54, v113\n"
      "v_mfma_f32_16x16x32_bf16 v[196:199], v[56:59], v[80:83], v[196:199]\n"
      "s_waitcnt lgkmcnt(7)\n"
      "ds_read_b64_tr_b16 v[78:79], v122 offset:25088\n"
      "s_or_b64 s[60:61], s[60:61], vcc\n"
      "ds_bpermute_b32 v12, v124, v102 offset:0\n"
      "v_mfma_f32_16x16x32_bf16 v[196:199], v[8:11], v[88:91], v[196:199]\n"
      "ds_read_b64_tr_b16 v[80:81], v123 offset:512\n"
      "ds_bpermute_b32 v13, v124, v102 offset:4\n"
      "v_mfma_f32_16x16x32_bf16 v[196:199], v[40:43], v[92:95], v[196:199]\n"
      "ds_read_b64_tr_b16 v[84:85], v123 offset:16896\n"
      "ds_bpermute_b32 v14, v124, v102 offset:8\n"
      "ds_bpermute_b32 v15, v124, v102 offset:12\n"
      "v_mfma_f32_16x16x32_bf16 v[132:135], v[32:35], v[92:95], v[132:135]\n"
      "ds_read_b64_tr_b16 v[82:83], v123 offset:8704\n"
      "ds_bpermute_b32 v44, v124, v103 offset:0\n"
      "ds_bpermute_b32 v45, v124, v103 offset:4\n"
      "v_mfma_f32_16x16x32_bf16 v[132:135], v[0:3], v[88:91], v[132:135]\n"
      "s_waitcnt lgkmcnt(13)\n"
      "ds_read_b64_tr_b16 v[86:87], v123 offset:25088\n"
      "ds_bpermute_b32 v46, v124, v103 offset:8\n"
      "ds_bpermute_b32 v47, v124, v103 offset:12\n"
      "v_mfma_f32_16x16x32_bf16 v[136:139], v[0:3], v[64:67], v[136:139]\n"
      "ds_read_b64_tr_b16 v[88:89], v118 offset:512\n"
      "v_mfma_f32_16x16x32_bf16 v[136:139], v[32:35], v[68:71], v[136:139]\n"
      "ds_read_b64_tr_b16 v[92:93], v118 offset:16896\n"
      "v_mfma_f32_16x16x32_bf16 v[200:203], v[40:43], v[68:71], v[200:203]\n"
      "ds_read_b64_tr_b16 v[90:91], v118 offset:8704\n"
      "v_mfma_f32_16x16x32_bf16 v[200:203], v[8:11], v[64:67], v[200:203]\n"
      "s_waitcnt lgkmcnt(15)\n"
      "ds_read_b64_tr_b16 v[94:95], v118 offset:25088\n"
      "v_mfma_f32_16x16x32_bf16 v[200:203], v[56:59], v[72:75], v[200:203]\n"
      "ds_read_b64_tr_b16 v[64:65], v117 offset:1024\n"
      "s_waitcnt lgkmcnt(11)\n"
      "v_mul_f32 v128, v12, v128\n"
      "v_mfma_f32_16x16x32_bf16 v[200:203], v[36:39], v[76:79], v[200:203]\n"
      "ds_read_b64_tr_b16 v[68:69], v117 offset:17408\n"
      "v_mul_f32 v129, v13, v129\n"
      "v_mul_f32 v130, v14, v130\n"
      "v_mfma_f32_16x16x32_bf16 v[136:139], v[4:7], v[76:79], v[136:139]\n"
      "ds_read_b64_tr_b16 v[66:67], v117 offset:9216\n"
      "v_mul_f32 v131, v15, v131\n"
      "v_mul_f32 v132, v12, v132\n"
      "v_mfma_f32_16x16x32_bf16 v[136:139], v[48:51], v[72:75], v[136:139]\n"
      "s_waitcnt lgkmcnt(9)\n"
      "ds_read_b64_tr_b16 v[70:71], v117 offset:25600\n"
      "v_mul_f32 v133, v13, v133\n"
      "v_mfma_f32_16x16x32_bf16 v[140:143], v[48:51], v[80:83], v[140:143]\n"
      "ds_read_b64_tr_b16 v[72:73], v122 offset:1024\n"
      "v_mul_f32 v134, v14, v134\n"
      "v_mul_f32 v135, v15, v135\n"
      "v_mfma_f32_16x16x32_bf16 v[140:143], v[4:7], v[84:87], v[140:143]\n"
      "ds_read_b64_tr_b16 v[76:77], v122 offset:17408\n"
      "v_cvt_pk_bf16_f32 v128, v128, v129\n"
      "v_cvt_pk_bf16_f32 v129, v130, v131\n"
      "v_mfma_f32_16x16x32_bf16 v[204:207], v[36:39], v[84:87], v[204:207]\n"
      "ds_read_b64_tr_b16 v[74:75], v122 offset:9216\n"
      "v_cvt_pk_bf16_f32 v130, v132, v133\n"
      "v_mfma_f32_16x16x32_bf16 v[204:207], v[56:59], v[80:83], v[204:207]\n"
      "s_waitcnt lgkmcnt(7)\n"
      "ds_read_b64_tr_b16 v[78:79], v122 offset:25600\n"
      "v_cvt_pk_bf16_f32 v131, v134, v135\n"
      "ds_write_b64 v119, v[128:129] offset:0\n"
      "v_mfma_f32_16x16x32_bf16 v[204:207], v[8:11], v[88:91], v[204:207]\n"
      "ds_read_b64_tr_b16 v[80:81], v123 offset:1024\n"
      "ds_write_b64 v119, v[130:131] offset:512\n"
      "ds_read_b64_tr_b16 v[132:133], v120 offset:0\n"
      "v_mfma_f32_16x16x32_bf16 v[204:207], v[40:43], v[92:95], v[204:207]\n"
      "ds_read_b64_tr_b16 v[84:85], v123 offset:17408\n"
      "ds_read_b64_tr_b16 v[134:135], v121 offset:0\n"
      "v_mfma_f32_16x16x32_bf16 v[140:143], v[32:35], v[92:95], v[140:143]\n"
      "ds_read_b64_tr_b16 v[82:83], v123 offset:9216\n"
      "v_mul_f32 v192, v44, v192\n"
      "v_mul_f32 v193, v45, v193\n"
      "v_mfma_f32_16x16x32_bf16 v[140:143], v[0:3], v[88:91], v[140:143]\n"
      "s_waitcnt lgkmcnt(11)\n"
      "ds_read_b64_tr_b16 v[86:87], v123 offset:25600\n"
      "v_mul_f32 v194, v46, v194\n"
      "v_mul_f32 v195, v47, v195\n"
      "v_mfma_f32_16x16x32_bf16 v[144:147], v[0:3], v[64:67], v[144:147]\n"
      "ds_read_b64_tr_b16 v[88:89], v118 offset:1024\n"
      "v_mul_f32 v196, v44, v196\n"
      "v_mfma_f32_16x16x32_bf16 v[144:147], v[32:35], v[68:71], v[144:147]\n"
      "ds_read_b64_tr_b16 v[92:93], v118 offset:17408\n"
      "v_mul_f32 v197, v45, v197\n"
      "v_mul_f32 v198, v46, v198\n"
      "v_mfma_f32_16x16x32_bf16 v[208:211], v[40:43], v[68:71], v[208:211]\n"
      "ds_read_b64_tr_b16 v[90:91], v118 offset:9216\n"
      "v_mul_f32 v199, v47, v199\n"
      "s_waitcnt lgkmcnt(5)\n"
      "buffer_store_dwordx4 v[132:135], v125, s[56:59], 0 offen offset:0 sc0 sc1 nt\n"
      "v_mfma_f32_16x16x32_bf16 v[208:211], v[8:11], v[64:67], v[208:211]\n"
      "ds_read_b64_tr_b16 v[94:95], v118 offset:25600\n"
      "v_cvt_pk_bf16_f32 v192, v192, v193\n"
      "v_mfma_f32_16x16x32_bf16 v[208:211], v[56:59], v[72:75], v[208:211]\n"
      "ds_read_b64_tr_b16 v[64:65], v117 offset:1536\n"
      "v_cvt_pk_bf16_f32 v193, v194, v195\n"
      "v_cvt_pk_bf16_f32 v194, v196, v197\n"
      "v_mfma_f32_16x16x32_bf16 v[208:211], v[36:39], v[76:79], v[208:211]\n"
      "ds_read_b64_tr_b16 v[68:69], v117 offset:17920\n"
      "v_cvt_pk_bf16_f32 v195, v198, v199\n"
      "ds_write_b64 v119, v[192:193] offset:1024\n"
      "v_mfma_f32_16x16x32_bf16 v[144:147], v[4:7], v[76:79], v[144:147]\n"
      "ds_read_b64_tr_b16 v[66:67], v117 offset:9728\n"
      "ds_write_b64 v119, v[194:195] offset:1536\n"
      "v_mfma_f32_16x16x32_bf16 v[144:147], v[48:51], v[72:75], v[144:147]\n"
      "s_waitcnt lgkmcnt(9)\n"
      "ds_read_b64_tr_b16 v[70:71], v117 offset:26112\n"
      "ds_read_b64_tr_b16 v[196:197], v120 offset:1024\n"
      "ds_read_b64_tr_b16 v[198:199], v121 offset:1024\n"
      "v_mfma_f32_16x16x32_bf16 v[148:151], v[48:51], v[80:83], v[148:151]\n"
      "ds_read_b64_tr_b16 v[72:73], v122 offset:1536\n"
      "v_mul_f32 v136, v12, v136\n"
      "v_mul_f32 v137, v13, v137\n"
      "v_mfma_f32_16x16x32_bf16 v[148:151], v[4:7], v[84:87], v[148:151]\n"
      "ds_read_b64_tr_b16 v[76:77], v122 offset:17920\n"
      "v_mul_f32 v138, v14, v138\n"
      "v_mfma_f32_16x16x32_bf16 v[212:215], v[36:39], v[84:87], v[212:215]\n"
      "ds_read_b64_tr_b16 v[74:75], v122 offset:9728\n"
      "v_mul_f32 v139, v15, v139\n"
      "v_mul_f32 v140, v12, v140\n"
      "v_mfma_f32_16x16x32_bf16 v[212:215], v[56:59], v[80:83], v[212:215]\n"
      "s_waitcnt lgkmcnt(11)\n"
      "ds_read_b64_tr_b16 v[78:79], v122 offset:26112\n"
      "v_mul_f32 v141, v13, v141\n"
      "v_mul_f32 v142, v14, v142\n"
      "v_mfma_f32_16x16x32_bf16 v[212:215], v[8:11], v[88:91], v[212:215]\n"
      "ds_read_b64_tr_b16 v[80:81], v123 offset:1536\n"
      "v_mul_f32 v143, v15, v143\n"
      "v_mfma_f32_16x16x32_bf16 v[212:215], v[40:43], v[92:95], v[212:215]\n"
      "ds_read_b64_tr_b16 v[84:85], v123 offset:17920\n"
      "s_waitcnt lgkmcnt(6)\n"
      "buffer_store_dwordx4 v[196:199], v96, s[56:59], 0 offen offset:0 sc0 sc1 nt\n"
      "v_cvt_pk_bf16_f32 v136, v136, v137\n"
      "v_mfma_f32_16x16x32_bf16 v[148:151], v[32:35], v[92:95], v[148:151]\n"
      "ds_read_b64_tr_b16 v[82:83], v123 offset:9728\n"
      "v_cvt_pk_bf16_f32 v137, v138, v139\n"
      "v_cvt_pk_bf16_f32 v138, v140, v141\n"
      "v_mfma_f32_16x16x32_bf16 v[148:151], v[0:3], v[88:91], v[148:151]\n"
      "ds_read_b64_tr_b16 v[86:87], v123 offset:26112\n"
      "v_cvt_pk_bf16_f32 v139, v142, v143\n"
      "v_mfma_f32_16x16x32_bf16 v[152:155], v[0:3], v[64:67], v[152:155]\n"
      "ds_read_b64_tr_b16 v[88:89], v118 offset:1536\n"
      "ds_write_b64 v119, v[136:137] offset:2048\n"
      "ds_write_b64 v119, v[138:139] offset:2560\n"
      "v_mfma_f32_16x16x32_bf16 v[152:155], v[32:35], v[68:71], v[152:155]\n"
      "ds_read_b64_tr_b16 v[92:93], v118 offset:17920\n"
      "ds_read_b64_tr_b16 v[140:141], v120 offset:2048\n"
      "ds_read_b64_tr_b16 v[142:143], v121 offset:2048\n"
      "v_mfma_f32_16x16x32_bf16 v[216:219], v[40:43], v[68:71], v[216:219]\n"
      "ds_read_b64_tr_b16 v[90:91], v118 offset:9728\n"
      "v_mul_f32 v200, v44, v200\n"
      "v_mfma_f32_16x16x32_bf16 v[216:219], v[8:11], v[64:67], v[216:219]\n"
      "s_waitcnt lgkmcnt(11)\n"
      "ds_read_b64_tr_b16 v[94:95], v118 offset:26112\n"
      "v_mul_f32 v201, v45, v201\n"
      "v_mul_f32 v202, v46, v202\n"
      "v_mfma_f32_16x16x32_bf16 v[216:219], v[56:59], v[72:75], v[216:219]\n"
      "ds_read_b64_tr_b16 v[64:65], v117 offset:2048\n"
      "v_mul_f32 v203, v47, v203\n"
      "v_mul_f32 v204, v44, v204\n"
      "v_mfma_f32_16x16x32_bf16 v[216:219], v[36:39], v[76:79], v[216:219]\n"
      "ds_read_b64_tr_b16 v[68:69], v117 offset:18432\n"
      "v_mul_f32 v205, v45, v205\n"
      "v_mfma_f32_16x16x32_bf16 v[152:155], v[4:7], v[76:79], v[152:155]\n"
      "ds_read_b64_tr_b16 v[66:67], v117 offset:10240\n"
      "v_mul_f32 v206, v46, v206\n"
      "v_mul_f32 v207, v47, v207\n"
      "v_mfma_f32_16x16x32_bf16 v[152:155], v[48:51], v[72:75], v[152:155]\n"
      "s_waitcnt lgkmcnt(11)\n"
      "ds_read_b64_tr_b16 v[70:71], v117 offset:26624\n"
      "s_waitcnt lgkmcnt(6)\n"
      "buffer_store_dwordx4 v[140:143], v125, s[56:59], 0 offen offset:64 sc0 sc1 nt\n"
      "v_cvt_pk_bf16_f32 v200, v200, v201\n"
      "v_mfma_f32_16x16x32_bf16 v[156:159], v[48:51], v[80:83], v[156:159]\n"
      "ds_read_b64_tr_b16 v[72:73], v122 offset:2048\n"
      "v_cvt_pk_bf16_f32 v201, v202, v203\n"
      "v_mfma_f32_16x16x32_bf16 v[156:159], v[4:7], v[84:87], v[156:159]\n"
      "ds_read_b64_tr_b16 v[76:77], v122 offset:18432\n"
      "v_cvt_pk_bf16_f32 v202, v204, v205\n"
      "v_cvt_pk_bf16_f32 v203, v206, v207\n"
      "v_mfma_f32_16x16x32_bf16 v[220:223], v[36:39], v[84:87], v[220:223]\n"
      "ds_read_b64_tr_b16 v[74:75], v122 offset:10240\n"
      "ds_write_b64 v119, v[200:201] offset:3072\n"
      "ds_write_b64 v119, v[202:203] offset:3584\n"
      "v_mfma_f32_16x16x32_bf16 v[220:223], v[56:59], v[80:83], v[220:223]\n"
      "s_waitcnt lgkmcnt(9)\n"
      "ds_read_b64_tr_b16 v[78:79], v122 offset:26624\n"
      "ds_read_b64_tr_b16 v[204:205], v120 offset:3072\n"
      "v_mfma_f32_16x16x32_bf16 v[220:223], v[8:11], v[88:91], v[220:223]\n"
      "ds_read_b64_tr_b16 v[80:81], v123 offset:2048\n"
      "ds_read_b64_tr_b16 v[206:207], v121 offset:3072\n"
      "v_mul_f32 v144, v12, v144\n"
      "v_mfma_f32_16x16x32_bf16 v[220:223], v[40:43], v[92:95], v[220:223]\n"
      "ds_read_b64_tr_b16 v[84:85], v123 offset:18432\n"
      "v_mul_f32 v145, v13, v145\n"
      "v_mul_f32 v146, v14, v146\n"
      "v_mfma_f32_16x16x32_bf16 v[156:159], v[32:35], v[92:95], v[156:159]\n"
      "ds_read_b64_tr_b16 v[82:83], v123 offset:10240\n"
      "v_mul_f32 v147, v15, v147\n"
      "v_mfma_f32_16x16x32_bf16 v[156:159], v[0:3], v[88:91], v[156:159]\n"
      "s_waitcnt lgkmcnt(11)\n"
      "ds_read_b64_tr_b16 v[86:87], v123 offset:26624\n"
      "v_mul_f32 v148, v12, v148\n"
      "v_mul_f32 v149, v13, v149\n"
      "v_mfma_f32_16x16x32_bf16 v[160:163], v[0:3], v[64:67], v[160:163]\n"
      "ds_read_b64_tr_b16 v[88:89], v118 offset:2048\n"
      "v_mul_f32 v150, v14, v150\n"
      "v_mul_f32 v151, v15, v151\n"
      "v_mfma_f32_16x16x32_bf16 v[160:163], v[32:35], v[68:71], v[160:163]\n"
      "ds_read_b64_tr_b16 v[92:93], v118 offset:18432\n"
      "s_waitcnt lgkmcnt(5)\n"
      "buffer_store_dwordx4 v[204:207], v96, s[56:59], 0 offen offset:64 sc0 sc1 nt\n"
      "v_mfma_f32_16x16x32_bf16 v[224:227], v[40:43], v[68:71], v[224:227]\n"
      "ds_read_b64_tr_b16 v[90:91], v118 offset:10240\n"
      "v_cvt_pk_bf16_f32 v144, v144, v145\n"
      "v_cvt_pk_bf16_f32 v145, v146, v147\n"
      "v_mfma_f32_16x16x32_bf16 v[224:227], v[8:11], v[64:67], v[224:227]\n"
      "ds_read_b64_tr_b16 v[94:95], v118 offset:26624\n"
      "v_cvt_pk_bf16_f32 v146, v148, v149\n"
      "v_cvt_pk_bf16_f32 v147, v150, v151\n"
      "v_mfma_f32_16x16x32_bf16 v[224:227], v[56:59], v[72:75], v[224:227]\n"
      "ds_read_b64_tr_b16 v[64:65], v117 offset:2560\n"
      "ds_write_b64 v119, v[144:145] offset:4096\n"
      "v_mfma_f32_16x16x32_bf16 v[224:227], v[36:39], v[76:79], v[224:227]\n"
      "ds_read_b64_tr_b16 v[68:69], v117 offset:18944\n"
      "ds_write_b64 v119, v[146:147] offset:4608\n"
      "ds_read_b64_tr_b16 v[148:149], v120 offset:4096\n"
      "v_mfma_f32_16x16x32_bf16 v[160:163], v[4:7], v[76:79], v[160:163]\n"
      "ds_read_b64_tr_b16 v[66:67], v117 offset:10752\n"
      "ds_read_b64_tr_b16 v[150:151], v121 offset:4096\n"
      "v_mul_f32 v208, v44, v208\n"
      "v_mfma_f32_16x16x32_bf16 v[160:163], v[48:51], v[72:75], v[160:163]\n"
      "s_waitcnt lgkmcnt(11)\n"
      "ds_read_b64_tr_b16 v[70:71], v117 offset:27136\n"
      "v_mul_f32 v209, v45, v209\n"
      "v_mfma_f32_16x16x32_bf16 v[164:167], v[48:51], v[80:83], v[164:167]\n"
      "ds_read_b64_tr_b16 v[72:73], v122 offset:2560\n"
      "v_mul_f32 v210, v46, v210\n"
      "v_mul_f32 v211, v47, v211\n"
      "v_mfma_f32_16x16x32_bf16 v[164:167], v[4:7], v[84:87], v[164:167]\n"
      "ds_read_b64_tr_b16 v[76:77], v122 offset:18944\n"
      "v_mul_f32 v212, v44, v212\n"
      "v_mul_f32 v213, v45, v213\n"
      "v_mfma_f32_16x16x32_bf16 v[228:231], v[36:39], v[84:87], v[228:231]\n"
      "ds_read_b64_tr_b16 v[74:75], v122 offset:10752\n"
      "v_mul_f32 v214, v46, v214\n"
      "v_mfma_f32_16x16x32_bf16 v[228:231], v[56:59], v[80:83], v[228:231]\n"
      "s_waitcnt lgkmcnt(11)\n"
      "ds_read_b64_tr_b16 v[78:79], v122 offset:27136\n"
      "v_mul_f32 v215, v47, v215\n"
      "s_waitcnt lgkmcnt(5)\n"
      "buffer_store_dwordx4 v[148:151], v125, s[56:59], 0 offen offset:128 sc0 sc1 nt\n"
      "v_mfma_f32_16x16x32_bf16 v[228:231], v[8:11], v[88:91], v[228:231]\n"
      "ds_read_b64_tr_b16 v[80:81], v123 offset:2560\n"
      "v_cvt_pk_bf16_f32 v208, v208, v209\n"
      "v_cvt_pk_bf16_f32 v209, v210, v211\n"
      "v_mfma_f32_16x16x32_bf16 v[228:231], v[40:43], v[92:95], v[228:231]\n"
      "ds_read_b64_tr_b16 v[84:85], v123 offset:18944\n"
      "v_cvt_pk_bf16_f32 v210, v212, v213\n"
      "v_mfma_f32_16x16x32_bf16 v[164:167], v[32:35], v[92:95], v[164:167]\n"
      "ds_read_b64_tr_b16 v[82:83], v123 offset:10752\n"
      "v_cvt_pk_bf16_f32 v211, v214, v215\n"
      "ds_write_b64 v119, v[208:209] offset:5120\n"
      "v_mfma_f32_16x16x32_bf16 v[164:167], v[0:3], v[88:91], v[164:167]\n"
      "s_waitcnt lgkmcnt(8)\n"
      "ds_read_b64_tr_b16 v[86:87], v123 offset:27136\n"
      "ds_write_b64 v119, v[210:211] offset:5632\n"
      "ds_read_b64_tr_b16 v[212:213], v120 offset:5120\n"
      "v_mfma_f32_16x16x32_bf16 v[168:171], v[0:3], v[64:67], v[168:171]\n"
      "ds_read_b64_tr_b16 v[88:89], v118 offset:2560\n"
      "ds_read_b64_tr_b16 v[214:215], v121 offset:5120\n"
      "v_mfma_f32_16x16x32_bf16 v[168:171], v[32:35], v[68:71], v[168:171]\n"
      "ds_read_b64_tr_b16 v[92:93], v118 offset:18944\n"
      "v_mul_f32 v152, v12, v152\n"
      "v_mul_f32 v153, v13, v153\n"
      "v_mfma_f32_16x16x32_bf16 v[232:235], v[40:43], v[68:71], v[232:235]\n"
      "ds_read_b64_tr_b16 v[90:91], v118 offset:10752\n"
      "v_mul_f32 v154, v14, v154\n"
      "v_mul_f32 v155, v15, v155\n"
      "v_mfma_f32_16x16x32_bf16 v[232:235], v[8:11], v[64:67], v[232:235]\n"
      "s_waitcnt lgkmcnt(11)\n"
      "ds_read_b64_tr_b16 v[94:95], v118 offset:27136\n"
      "v_mul_f32 v156, v12, v156\n"
      "v_mfma_f32_16x16x32_bf16 v[232:235], v[56:59], v[72:75], v[232:235]\n"
      "ds_read_b64_tr_b16 v[64:65], v117 offset:3072\n"
      "v_mul_f32 v157, v13, v157\n"
      "v_mul_f32 v158, v14, v158\n"
      "v_mfma_f32_16x16x32_bf16 v[232:235], v[36:39], v[76:79], v[232:235]\n"
      "ds_read_b64_tr_b16 v[68:69], v117 offset:19456\n"
      "v_mul_f32 v159, v15, v159\n"
      "s_waitcnt lgkmcnt(5)\n"
      "buffer_store_dwordx4 v[212:215], v96, s[56:59], 0 offen offset:128 sc0 sc1 nt\n"
      "v_mfma_f32_16x16x32_bf16 v[168:171], v[4:7], v[76:79], v[168:171]\n"
      "ds_read_b64_tr_b16 v[66:67], v117 offset:11264\n"
      "v_cvt_pk_bf16_f32 v152, v152, v153\n"
      "v_mfma_f32_16x16x32_bf16 v[168:171], v[48:51], v[72:75], v[168:171]\n"
      "ds_read_b64_tr_b16 v[70:71], v117 offset:27648\n"
      "v_cvt_pk_bf16_f32 v153, v154, v155\n"
      "v_cvt_pk_bf16_f32 v154, v156, v157\n"
      "v_mfma_f32_16x16x32_bf16 v[172:175], v[48:51], v[80:83], v[172:175]\n"
      "ds_read_b64_tr_b16 v[72:73], v122 offset:3072\n"
      "v_cvt_pk_bf16_f32 v155, v158, v159\n"
      "ds_write_b64 v119, v[152:153] offset:6144\n"
      "v_mfma_f32_16x16x32_bf16 v[172:175], v[4:7], v[84:87], v[172:175]\n"
      "ds_read_b64_tr_b16 v[76:77], v122 offset:19456\n"
      "ds_write_b64 v119, v[154:155] offset:6656\n"
      "v_mfma_f32_16x16x32_bf16 v[236:239], v[36:39], v[84:87], v[236:239]\n"
      "ds_read_b64_tr_b16 v[74:75], v122 offset:11264\n"
      "ds_read_b64_tr_b16 v[156:157], v120 offset:6144\n"
      "ds_read_b64_tr_b16 v[158:159], v121 offset:6144\n"
      "v_mfma_f32_16x16x32_bf16 v[236:239], v[56:59], v[80:83], v[236:239]\n"
      "s_waitcnt lgkmcnt(11)\n"
      "ds_read_b64_tr_b16 v[78:79], v122 offset:27648\n"
      "v_mul_f32 v216, v44, v216\n"
      "v_mul_f32 v217, v45, v217\n"
      "v_mfma_f32_16x16x32_bf16 v[236:239], v[8:11], v[88:91], v[236:239]\n"
      "ds_read_b64_tr_b16 v[80:81], v123 offset:3072\n"
      "v_mul_f32 v218, v46, v218\n"
      "v_mfma_f32_16x16x32_bf16 v[236:239], v[40:43], v[92:95], v[236:239]\n"
      "ds_read_b64_tr_b16 v[84:85], v123 offset:19456\n"
      "v_mul_f32 v219, v47, v219\n"
      "v_mul_f32 v220, v44, v220\n"
      "v_mfma_f32_16x16x32_bf16 v[172:175], v[32:35], v[92:95], v[172:175]\n"
      "ds_read_b64_tr_b16 v[82:83], v123 offset:11264\n"
      "v_mul_f32 v221, v45, v221\n"
      "v_mul_f32 v222, v46, v222\n"
      "v_mfma_f32_16x16x32_bf16 v[172:175], v[0:3], v[88:91], v[172:175]\n"
      "s_waitcnt lgkmcnt(11)\n"
      "ds_read_b64_tr_b16 v[86:87], v123 offset:27648\n"
      "v_mul_f32 v223, v47, v223\n"
      "v_mfma_f32_16x16x32_bf16 v[176:179], v[0:3], v[64:67], v[176:179]\n"
      "ds_read_b64_tr_b16 v[88:89], v118 offset:3072\n"
      "s_waitcnt lgkmcnt(6)\n"
      "buffer_store_dwordx4 v[156:159], v125, s[56:59], 0 offen offset:192 sc0 sc1 nt\n"
      "v_cvt_pk_bf16_f32 v216, v216, v217\n"
      "v_mfma_f32_16x16x32_bf16 v[176:179], v[32:35], v[68:71], v[176:179]\n"
      "ds_read_b64_tr_b16 v[92:93], v118 offset:19456\n"
      "v_cvt_pk_bf16_f32 v217, v218, v219\n"
      "v_cvt_pk_bf16_f32 v218, v220, v221\n"
      "v_mfma_f32_16x16x32_bf16 v[240:243], v[40:43], v[68:71], v[240:243]\n"
      "ds_read_b64_tr_b16 v[90:91], v118 offset:11264\n"
      "v_cvt_pk_bf16_f32 v219, v222, v223\n"
      "v_mfma_f32_16x16x32_bf16 v[240:243], v[8:11], v[64:67], v[240:243]\n"
      "s_waitcnt lgkmcnt(7)\n"
      "ds_read_b64_tr_b16 v[94:95], v118 offset:27648\n"
      "ds_write_b64 v119, v[216:217] offset:7168\n"
      "ds_write_b64 v119, v[218:219] offset:7680\n"
      "v_mfma_f32_16x16x32_bf16 v[240:243], v[56:59], v[72:75], v[240:243]\n"
      "ds_read_b64_tr_b16 v[64:65], v117 offset:3584\n"
      "ds_read_b64_tr_b16 v[220:221], v120 offset:7168\n"
      "ds_read_b64_tr_b16 v[222:223], v121 offset:7168\n"
      "v_mfma_f32_16x16x32_bf16 v[240:243], v[36:39], v[76:79], v[240:243]\n"
      "ds_read_b64_tr_b16 v[68:69], v117 offset:19968\n"
      "v_mul_f32 v160, v12, v160\n"
      "v_mfma_f32_16x16x32_bf16 v[176:179], v[4:7], v[76:79], v[176:179]\n"
      "ds_read_b64_tr_b16 v[66:67], v117 offset:11776\n"
      "v_mul_f32 v161, v13, v161\n"
      "v_mul_f32 v162, v14, v162\n"
      "v_mfma_f32_16x16x32_bf16 v[176:179], v[48:51], v[72:75], v[176:179]\n"
      "s_waitcnt lgkmcnt(11)\n"
      "ds_read_b64_tr_b16 v[70:71], v117 offset:28160\n"
      "v_mul_f32 v163, v15, v163\n"
      "v_mul_f32 v164, v12, v164\n"
      "v_mfma_f32_16x16x32_bf16 v[180:183], v[48:51], v[80:83], v[180:183]\n"
      "ds_read_b64_tr_b16 v[72:73], v122 offset:3584\n"
      "v_mul_f32 v165, v13, v165\n"
      "v_mfma_f32_16x16x32_bf16 v[180:183], v[4:7], v[84:87], v[180:183]\n"
      "ds_read_b64_tr_b16 v[76:77], v122 offset:19968\n"
      "v_mul_f32 v166, v14, v166\n"
      "v_mul_f32 v167, v15, v167\n"
      "v_mfma_f32_16x16x32_bf16 v[244:247], v[36:39], v[84:87], v[244:247]\n"
      "ds_read_b64_tr_b16 v[74:75], v122 offset:11776\n"
      "s_waitcnt lgkmcnt(6)\n"
      "buffer_store_dwordx4 v[220:223], v96, s[56:59], 0 offen offset:192 sc0 sc1 nt\n"
      "v_cvt_pk_bf16_f32 v160, v160, v161\n"
      "v_mfma_f32_16x16x32_bf16 v[244:247], v[56:59], v[80:83], v[244:247]\n"
      "ds_read_b64_tr_b16 v[78:79], v122 offset:28160\n"
      "v_cvt_pk_bf16_f32 v161, v162, v163\n"
      "v_mfma_f32_16x16x32_bf16 v[244:247], v[8:11], v[88:91], v[244:247]\n"
      "ds_read_b64_tr_b16 v[80:81], v123 offset:3584\n"
      "v_cvt_pk_bf16_f32 v162, v164, v165\n"
      "v_cvt_pk_bf16_f32 v163, v166, v167\n"
      "v_mfma_f32_16x16x32_bf16 v[244:247], v[40:43], v[92:95], v[244:247]\n"
      "ds_read_b64_tr_b16 v[84:85], v123 offset:19968\n"
      "ds_write_b64 v119, v[160:161] offset:0\n"
      "ds_write_b64 v119, v[162:163] offset:512\n"
      "v_mfma_f32_16x16x32_bf16 v[180:183], v[32:35], v[92:95], v[180:183]\n"
      "ds_read_b64_tr_b16 v[82:83], v123 offset:11776\n"
      "ds_read_b64_tr_b16 v[164:165], v120 offset:0\n"
      "v_mfma_f32_16x16x32_bf16 v[180:183], v[0:3], v[88:91], v[180:183]\n"
      "s_waitcnt lgkmcnt(10)\n"
      "ds_read_b64_tr_b16 v[86:87], v123 offset:28160\n"
      "ds_read_b64_tr_b16 v[166:167], v121 offset:0\n"
      "v_mul_f32 v224, v44, v224\n"
      "v_mfma_f32_16x16x32_bf16 v[184:187], v[0:3], v[64:67], v[184:187]\n"
      "ds_read_b64_tr_b16 v[88:89], v118 offset:3584\n"
      "v_mul_f32 v225, v45, v225\n"
      "v_mul_f32 v226, v46, v226\n"
      "v_mfma_f32_16x16x32_bf16 v[184:187], v[32:35], v[68:71], v[184:187]\n"
      "ds_read_b64_tr_b16 v[92:93], v118 offset:19968\n"
      "v_mul_f32 v227, v47, v227\n"
      "v_mfma_f32_16x16x32_bf16 v[248:251], v[40:43], v[68:71], v[248:251]\n"
      "ds_read_b64_tr_b16 v[90:91], v118 offset:11776\n"
      "v_mul_f32 v228, v44, v228\n"
      "v_mul_f32 v229, v45, v229\n"
      "v_mfma_f32_16x16x32_bf16 v[248:251], v[8:11], v[64:67], v[248:251]\n"
      "s_waitcnt lgkmcnt(11)\n"
      "ds_read_b64_tr_b16 v[94:95], v118 offset:28160\n"
      "v_mul_f32 v230, v46, v230\n"
      "v_mul_f32 v231, v47, v231\n"
      "v_mfma_f32_16x16x32_bf16 v[248:251], v[56:59], v[72:75], v[248:251]\n"
      "ds_read_b128 v[64:67], v116 offset:32768\n"
      "s_waitcnt lgkmcnt(5)\n"
      "buffer_store_dwordx4 v[164:167], v125, s[56:59], 0 offen offset:256 sc0 sc1 nt\n"
      "v_mfma_f32_16x16x32_bf16 v[248:251], v[36:39], v[76:79], v[248:251]\n"
      "v_exp_f32 v20, v20\n"
      "v_cvt_pk_bf16_f32 v224, v224, v225\n"
      "v_mfma_f32_16x16x32_bf16 v[184:187], v[4:7], v[76:79], v[184:187]\n"
      "ds_read_b128 v[68:71], v116 offset:33280\n"
      "v_add_f32 v114, v114, v20\n"
      "v_cvt_pk_bf16_f32 v225, v226, v227\n"
      "v_mfma_f32_16x16x32_bf16 v[184:187], v[48:51], v[72:75], v[184:187]\n"
      "v_exp_f32 v21, v21\n"
      "v_mfma_f32_16x16x32_bf16 v[188:191], v[48:51], v[80:83], v[188:191]\n"
      "ds_read_b128 v[72:75], v116 offset:33792\n"
      "v_add_f32 v114, v114, v21\n"
      "v_cvt_pk_bf16_f32 v226, v228, v229\n"
      "v_mfma_f32_16x16x32_bf16 v[188:191], v[4:7], v[84:87], v[188:191]\n"
      "v_exp_f32 v22, v22\n"
      "v_cvt_pk_bf16_f32 v227, v230, v231\n"
      "v_mfma_f32_16x16x32_bf16 v[252:255], v[36:39], v[84:87], v[252:255]\n"
      "ds_read_b128 v[76:79], v116 offset:34304\n"
      "v_cvt_pk_bf16_f32 v20, v20, v21\n"
      "v_mfma_f32_16x16x32_bf16 v[252:255], v[56:59], v[80:83], v[252:255]\n"
      "s_waitcnt lgkmcnt(4)\n"
      "v_exp_f32 v23, v23\n"
      "ds_write_b64 v119, v[224:225] offset:1024\n"
      "v_mfma_f32_16x16x32_bf16 v[252:255], v[8:11], v[88:91], v[252:255]\n"
      "ds_read_b128 v[80:83], v116 offset:34816\n"
      "v_add_f32 v114, v114, v22\n"
      "ds_write_b64 v119, v[226:227] offset:1536\n"
      "v_mfma_f32_16x16x32_bf16 v[252:255], v[40:43], v[92:95], v[252:255]\n"
      "v_exp_f32 v28, v28\n"
      "v_mfma_f32_16x16x32_bf16 v[188:191], v[32:35], v[92:95], v[188:191]\n"
      "ds_read_b128 v[84:87], v116 offset:35328\n"
      "v_add_f32 v114, v114, v23\n"
      "ds_read_b64_tr_b16 v[228:229], v120 offset:1024\n"
      "v_mfma_f32_16x16x32_bf16 v[188:191], v[0:3], v[88:91], v[188:191]\n"
      "s_waitcnt lgkmcnt(7)\n"
      "v_exp_f32 v29, v29\n"
      "ds_read_b64_tr_b16 v[230:231], v121 offset:1024\n"
      "v_mfma_f32_16x16x32_bf16 v[0:3], v[64:67], a[64:67], v[104:107]\n"
      "ds_read_b128 v[88:91], v116 offset:35840\n"
      "v_cvt_pk_bf16_f32 v21, v22, v23\n"
      "v_mul_f32 v168, v12, v168\n"
      "v_mfma_f32_16x16x32_bf16 v[0:3], v[68:71], a[68:71], v[0:3]\n"
      "v_exp_f32 v30, v30\n"
      "v_mul_f32 v169, v13, v169\n"
      "v_mul_f32 v170, v14, v170\n"
      "v_mfma_f32_16x16x32_bf16 v[32:35], v[68:71], a[100:103], v[108:111]\n"
      "ds_read_b128 v[92:95], v116 offset:36352\n"
      "v_add_f32 v115, v115, v28\n"
      "v_mul_f32 v171, v15, v171\n"
      "v_mfma_f32_16x16x32_bf16 v[32:35], v[64:67], a[96:99], v[32:35]\n"
      "s_waitcnt lgkmcnt(8)\n"
      "v_exp_f32 v31, v31\n"
      "v_mul_f32 v172, v12, v172\n"
      "v_mul_f32 v173, v13, v173\n"
      "v_mfma_f32_16x16x32_bf16 v[32:35], v[72:75], a[104:107], v[32:35]\n"
      "ds_read_b128 v[64:67], v116 offset:40960\n"
      "v_add_f32 v115, v115, v29\n"
      "v_mul_f32 v174, v14, v174\n"
      "v_mfma_f32_16x16x32_bf16 v[32:35], v[76:79], a[108:111], v[32:35]\n"
      "v_exp_f32 v52, v52\n"
      "v_mul_f32 v175, v15, v175\n"
      "s_waitcnt lgkmcnt(3)\n"
      "buffer_store_dwordx4 v[228:231], v96, s[56:59], 0 offen offset:256 sc0 sc1 nt\n"
      "v_mfma_f32_16x16x32_bf16 v[0:3], v[76:79], a[76:79], v[0:3]\n"
      "ds_read_b128 v[68:71], v116 offset:41472\n"
      "v_cvt_pk_bf16_f32 v28, v28, v29\n"
      "v_cvt_pk_bf16_f32 v168, v168, v169\n"
      "v_cvt_pk_bf16_f32 v169, v170, v171\n"
      "v_mfma_f32_16x16x32_bf16 v[0:3], v[72:75], a[72:75], v[0:3]\n"
      "v_exp_f32 v53, v53\n"
      "v_cvt_pk_bf16_f32 v170, v172, v173\n"
      "v_mfma_f32_16x16x32_bf16 v[0:3], v[80:83], a[80:83], v[0:3]\n"
      "ds_read_b128 v[72:75], v116 offset:41984\n"
      "v_add_f32 v115, v115, v30\n"
      "v_cvt_pk_bf16_f32 v171, v174, v175\n"
      "ds_write_b64 v119, v[168:169] offset:2048\n"
      "v_mfma_f32_16x16x32_bf16 v[0:3], v[84:87], a[84:87], v[0:3]\n"
      "v_exp_f32 v54, v54\n"
      "ds_write_b64 v119, v[170:171] offset:2560\n"
      "v_mfma_f32_16x16x32_bf16 v[32:35], v[84:87], a[116:119], v[32:35]\n"
      "ds_read_b128 v[76:79], v116 offset:42496\n"
      "v_add_f32 v115, v115, v31\n"
      "ds_read_b64_tr_b16 v[172:173], v120 offset:2048\n"
      "ds_read_b64_tr_b16 v[174:175], v121 offset:2048\n"
      "v_mfma_f32_16x16x32_bf16 v[32:35], v[80:83], a[112:115], v[32:35]\n"
      "s_waitcnt lgkmcnt(8)\n"
      "v_exp_f32 v55, v55\n"
      "v_mul_f32 v232, v44, v232\n"
      "v_mul_f32 v233, v45, v233\n"
      "v_mfma_f32_16x16x32_bf16 v[32:35], v[88:91], a[120:123], v[32:35]\n"
      "ds_read_b128 v[80:83], v116 offset:43008\n"
      "v_cvt_pk_bf16_f32 v29, v30, v31\n"
      "v_add_f32 v114, v114, v52\n"
      "v_mul_f32 v234, v46, v234\n"
      "v_mfma_f32_16x16x32_bf16 v[32:35], v[92:95], a[124:127], v[32:35]\n"
      "v_exp_f32 v60, v60\n"
      "v_mul_f32 v235, v47, v235\n"
      "v_mul_f32 v236, v44, v236\n"
      "v_mfma_f32_16x16x32_bf16 v[0:3], v[92:95], a[92:95], v[0:3]\n"
      "ds_read_b128 v[84:87], v116 offset:43520\n"
      "v_add_f32 v114, v114, v53\n"
      "v_mul_f32 v237, v45, v237\n"
      "v_mfma_f32_16x16x32_bf16 v[0:3], v[88:91], a[88:91], v[0:3]\n"
      "s_waitcnt lgkmcnt(8)\n"
      "v_exp_f32 v61, v61\n"
      "v_mul_f32 v238, v46, v238\n"
      "v_mul_f32 v239, v47, v239\n"
      "v_mfma_f32_16x16x32_bf16 v[8:11], v[64:67], a[64:67], v[104:107]\n"
      "ds_read_b128 v[88:91], v116 offset:44032\n"
      "v_cvt_pk_bf16_f32 v22, v52, v53\n"
      "v_add_f32 v114, v114, v54\n"
      "s_waitcnt lgkmcnt(3)\n"
      "buffer_store_dwordx4 v[172:175], v125, s[56:59], 0 offen offset:320 sc0 sc1 nt\n"
      "v_cvt_pk_bf16_f32 v232, v232, v233\n"
      "v_mfma_f32_16x16x32_bf16 v[8:11], v[68:71], a[68:71], v[8:11]\n"
      "v_exp_f32 v62, v62\n"
      "v_cvt_pk_bf16_f32 v233, v234, v235\n"
      "v_mfma_f32_16x16x32_bf16 v[40:43], v[68:71], a[100:103], v[108:111]\n"
      "ds_read_b128 v[92:95], v116 offset:44544\n"
      "v_add_f32 v114, v114, v55\n"
      "v_cvt_pk_bf16_f32 v234, v236, v237\n"
      "v_cvt_pk_bf16_f32 v235, v238, v239\n"
      "v_mfma_f32_16x16x32_bf16 v[40:43], v[64:67], a[96:99], v[40:43]\n"
      "v_exp_f32 v63, v63\n"
      "ds_write_b64 v119, v[232:233] offset:3072\n"
      "v_mfma_f32_16x16x32_bf16 v[40:43], v[72:75], a[104:107], v[40:43]\n"
      "ds_read_b128 v[64:67], v116 offset:49152\n"
      "v_cvt_pk_bf16_f32 v23, v54, v55\n"
      "v_add_f32 v115, v115, v60\n"
      "ds_write_b64 v119, v[234:235] offset:3584\n"
      "ds_read_b64_tr_b16 v[236:237], v120 offset:3072\n"
      "v_mfma_f32_16x16x32_bf16 v[40:43], v[76:79], a[108:111], v[40:43]\n"
      "v_exp_f32 v0, v0\n"
      "ds_read_b64_tr_b16 v[238:239], v121 offset:3072\n"
      "v_mul_f32 v176, v12, v176\n"
      "v_mfma_f32_16x16x32_bf16 v[8:11], v[76:79], a[76:79], v[8:11]\n"
      "ds_read_b128 v[68:71], v116 offset:49664\n"
      "v_add_f32 v115, v115, v61\n"
      "v_mul_f32 v177, v13, v177\n"
      "v_mfma_f32_16x16x32_bf16 v[8:11], v[72:75], a[72:75], v[8:11]\n"
      "s_waitcnt lgkmcnt(8)\n"
      "v_exp_f32 v1, v1\n"
      "v_mul_f32 v178, v14, v178\n"
      "v_mul_f32 v179, v15, v179\n"
      "v_mfma_f32_16x16x32_bf16 v[8:11], v[80:83], a[80:83], v[8:11]\n"
      "ds_read_b128 v[72:75], v116 offset:50176\n"
      "v_cvt_pk_bf16_f32 v30, v60, v61\n"
      "v_add_f32 v115, v115, v62\n"
      "v_mul_f32 v180, v12, v180\n"
      "v_mfma_f32_16x16x32_bf16 v[8:11], v[84:87], a[84:87], v[8:11]\n"
      "v_exp_f32 v2, v2\n"
      "v_mul_f32 v181, v13, v181\n"
      "v_mul_f32 v182, v14, v182\n"
      "v_mfma_f32_16x16x32_bf16 v[40:43], v[84:87], a[116:119], v[40:43]\n"
      "ds_read_b128 v[76:79], v116 offset:50688\n"
      "v_add_f32 v115, v115, v63\n"
      "v_mul_f32 v183, v15, v183\n"
      "s_waitcnt lgkmcnt(3)\n"
      "buffer_store_dwordx4 v[236:239], v96, s[56:59], 0 offen offset:320 sc0 sc1 nt\n"
      "v_mfma_f32_16x16x32_bf16 v[40:43], v[80:83], a[112:115], v[40:43]\n"
      "v_exp_f32 v3, v3\n"
      "v_cvt_pk_bf16_f32 v176, v176, v177\n"
      "v_mfma_f32_16x16x32_bf16 v[40:43], v[88:91], a[120:123], v[40:43]\n"
      "ds_read_b128 v[80:83], v116 offset:51200\n"
      "v_cvt_pk_bf16_f32 v31, v62, v63\n"
      "v_add_f32 v114, v114, v0\n"
      "v_cvt_pk_bf16_f32 v177, v178, v179\n"
      "v_cvt_pk_bf16_f32 v178, v180, v181\n"
      "v_mfma_f32_16x16x32_bf16 v[40:43], v[92:95], a[124:127], v[40:43]\n"
      "v_exp_f32 v32, v32\n"
      "v_cvt_pk_bf16_f32 v179, v182, v183\n"
      "v_mfma_f32_16x16x32_bf16 v[8:11], v[92:95], a[92:95], v[8:11]\n"
      "ds_read_b128 v[84:87], v116 offset:51712\n"
      "v_add_f32 v114, v114, v1\n"
      "ds_write_b64 v119, v[176:177] offset:4096\n"
      "ds_write_b64 v119, v[178:179] offset:4608\n"
      "v_mfma_f32_16x16x32_bf16 v[8:11], v[88:91], a[88:91], v[8:11]\n"
      "s_waitcnt lgkmcnt(6)\n"
      "v_exp_f32 v33, v33\n"
      "ds_read_b64_tr_b16 v[180:181], v120 offset:4096\n"
      "ds_read_b64_tr_b16 v[182:183], v121 offset:4096\n"
      "v_mfma_f32_16x16x32_bf16 v[48:51], v[64:67], a[64:67], v[104:107]\n"
      "ds_read_b128 v[88:91], v116 offset:52224\n"
      "v_cvt_pk_bf16_f32 v0, v0, v1\n"
      "v_add_f32 v114, v114, v2\n"
      "v_mul_f32 v240, v44, v240\n"
      "v_mfma_f32_16x16x32_bf16 v[48:51], v[68:71], a[68:71], v[48:51]\n"
      "v_exp_f32 v34, v34\n"
      "v_mul_f32 v241, v45, v241\n"
      "v_mul_f32 v242, v46, v242\n"
      "v_mfma_f32_16x16x32_bf16 v[4:7], v[68:71], a[100:103], v[108:111]\n"
      "ds_read_b128 v[92:95], v116 offset:52736\n"
      "v_add_f32 v114, v114, v3\n"
      "v_mul_f32 v243, v47, v243\n"
      "v_mfma_f32_16x16x32_bf16 v[4:7], v[64:67], a[96:99], v[4:7]\n"
      "s_waitcnt lgkmcnt(8)\n"
      "v_exp_f32 v35, v35\n"
      "v_mul_f32 v244, v44, v244\n"
      "v_mul_f32 v245, v45, v245\n"
      "v_mfma_f32_16x16x32_bf16 v[4:7], v[72:75], a[104:107], v[4:7]\n"
      "ds_read_b128 v[64:67], v116 offset:57344\n"
      "v_cvt_pk_bf16_f32 v1, v2, v3\n"
      "v_add_f32 v115, v115, v32\n"
      "v_mul_f32 v246, v46, v246\n"
      "v_mfma_f32_16x16x32_bf16 v[4:7], v[76:79], a[108:111], v[4:7]\n"
      "v_exp_f32 v8, v8\n"
      "v_mul_f32 v247, v47, v247\n"
      "s_waitcnt lgkmcnt(3)\n"
      "buffer_store_dwordx4 v[180:183], v125, s[56:59], 0 offen offset:384 sc0 sc1 nt\n"
      "v_mfma_f32_16x16x32_bf16 v[48:51], v[76:79], a[76:79], v[48:51]\n"
      "ds_read_b128 v[68:71], v116 offset:57856\n"
      "v_add_f32 v115, v115, v33\n"
      "v_cvt_pk_bf16_f32 v240, v240, v241\n"
      "v_cvt_pk_bf16_f32 v241, v242, v243\n"
      "v_mfma_f32_16x16x32_bf16 v[48:51], v[72:75], a[72:75], v[48:51]\n"
      "v_exp_f32 v9, v9\n"
      "v_cvt_pk_bf16_f32 v242, v244, v245\n"
      "v_mfma_f32_16x16x32_bf16 v[48:51], v[80:83], a[80:83], v[48:51]\n"
      "ds_read_b128 v[72:75], v116 offset:58368\n"
      "v_cvt_pk_bf16_f32 v32, v32, v33\n"
      "v_add_f32 v115, v115, v34\n"
      "v_cvt_pk_bf16_f32 v243, v246, v247\n"
      "ds_write_b64 v119, v[240:241] offset:5120\n"
      "v_mfma_f32_16x16x32_bf16 v[48:51], v[84:87], a[84:87], v[48:51]\n"
      "v_exp_f32 v10, v10\n"
      "ds_write_b64 v119, v[242:243] offset:5632\n"
      "v_mfma_f32_16x16x32_bf16 v[4:7], v[84:87], a[116:119], v[4:7]\n"
      "ds_read_b128 v[76:79], v116 offset:58880\n"
      "v_add_f32 v115, v115, v35\n"
      "ds_read_b64_tr_b16 v[244:245], v120 offset:5120\n"
      "ds_read_b64_tr_b16 v[246:247], v121 offset:5120\n"
      "v_mfma_f32_16x16x32_bf16 v[4:7], v[80:83], a[112:115], v[4:7]\n"
      "s_waitcnt lgkmcnt(8)\n"
      "v_exp_f32 v11, v11\n"
      "v_mul_f32 v184, v12, v184\n"
      "v_mul_f32 v185, v13, v185\n"
      "v_mfma_f32_16x16x32_bf16 v[4:7], v[88:91], a[120:123], v[4:7]\n"
      "ds_read_b128 v[80:83], v116 offset:59392\n"
      "v_cvt_pk_bf16_f32 v33, v34, v35\n"
      "v_add_f32 v114, v114, v8\n"
      "v_mul_f32 v186, v14, v186\n"
      "v_mfma_f32_16x16x32_bf16 v[4:7], v[92:95], a[124:127], v[4:7]\n"
      "v_exp_f32 v40, v40\n"
      "v_mul_f32 v187, v15, v187\n"
      "v_mul_f32 v188, v12, v188\n"
      "v_mfma_f32_16x16x32_bf16 v[48:51], v[92:95], a[92:95], v[48:51]\n"
      "ds_read_b128 v[84:87], v116 offset:59904\n"
      "v_add_f32 v114, v114, v9\n"
      "v_mul_f32 v189, v13, v189\n"
      "v_mfma_f32_16x16x32_bf16 v[48:51], v[88:91], a[88:91], v[48:51]\n"
      "s_waitcnt lgkmcnt(8)\n"
      "v_exp_f32 v41, v41\n"
      "v_mul_f32 v190, v14, v190\n"
      "v_mul_f32 v191, v15, v191\n"
      "v_mfma_f32_16x16x32_bf16 v[56:59], v[64:67], a[64:67], v[104:107]\n"
      "ds_read_b128 v[88:91], v116 offset:60416\n"
      "v_cvt_pk_bf16_f32 v2, v8, v9\n"
      "v_add_f32 v114, v114, v10\n"
      "s_waitcnt lgkmcnt(3)\n"
      "buffer_store_dwordx4 v[244:247], v96, s[56:59], 0 offen offset:384 sc0 sc1 nt\n"
      "v_cvt_pk_bf16_f32 v184, v184, v185\n"
      "v_mfma_f32_16x16x32_bf16 v[56:59], v[68:71], a[68:71], v[56:59]\n"
      "v_exp_f32 v42, v42\n"
      "v_cvt_pk_bf16_f32 v185, v186, v187\n"
      "v_mfma_f32_16x16x32_bf16 v[36:39], v[68:71], a[100:103], v[108:111]\n"
      "ds_read_b128 v[92:95], v116 offset:60928\n"
      "v_add_f32 v114, v114, v11\n"
      "v_cvt_pk_bf16_f32 v186, v188, v189\n"
      "v_cvt_pk_bf16_f32 v187, v190, v191\n"
      "v_mfma_f32_16x16x32_bf16 v[36:39], v[64:67], a[96:99], v[36:39]\n"
      "v_exp_f32 v43, v43\n"
      "ds_write_b64 v119, v[184:185] offset:6144\n"
      "v_mfma_f32_16x16x32_bf16 v[36:39], v[72:75], a[104:107], v[36:39]\n"
      "ds_read_b64_tr_b16 v[64:65], v117 offset:0\n"
      "ds_read_b64_tr_b16 v[68:69], v117 offset:16384\n"
      "v_cvt_pk_bf16_f32 v3, v10, v11\n"
      "ds_write_b64 v119, v[186:187] offset:6656\n"
      "ds_read_b64_tr_b16 v[188:189], v120 offset:6144\n"
      "v_mfma_f32_16x16x32_bf16 v[36:39], v[76:79], a[108:111], v[36:39]\n"
      "v_exp_f32 v48, v48\n"
      "ds_read_b64_tr_b16 v[190:191], v121 offset:6144\n"
      "v_mul_f32 v248, v44, v248\n"
      "v_mfma_f32_16x16x32_bf16 v[56:59], v[76:79], a[76:79], v[56:59]\n"
      "ds_read_b64_tr_b16 v[66:67], v117 offset:8192\n"
      "ds_read_b64_tr_b16 v[70:71], v117 offset:24576\n"
      "v_add_f32 v115, v115, v40\n"
      "v_mul_f32 v249, v45, v249\n"
      "v_mfma_f32_16x16x32_bf16 v[56:59], v[72:75], a[72:75], v[56:59]\n"
      "s_waitcnt lgkmcnt(10)\n"
      "v_exp_f32 v49, v49\n"
      "v_mul_f32 v250, v46, v250\n"
      "v_mul_f32 v251, v47, v251\n"
      "v_mfma_f32_16x16x32_bf16 v[56:59], v[80:83], a[80:83], v[56:59]\n"
      "ds_read_b64_tr_b16 v[72:73], v122 offset:0\n"
      "ds_read_b64_tr_b16 v[76:77], v122 offset:16384\n"
      "v_add_f32 v115, v115, v41\n"
      "v_mul_f32 v252, v44, v252\n"
      "v_mfma_f32_16x16x32_bf16 v[56:59], v[84:87], a[84:87], v[56:59]\n"
      "v_exp_f32 v50, v50\n"
      "v_mul_f32 v253, v45, v253\n"
      "v_mul_f32 v254, v46, v254\n"
      "v_mfma_f32_16x16x32_bf16 v[36:39], v[84:87], a[116:119], v[36:39]\n"
      "ds_read_b64_tr_b16 v[74:75], v122 offset:8192\n"
      "ds_read_b64_tr_b16 v[78:79], v122 offset:24576\n"
      "v_cvt_pk_bf16_f32 v34, v40, v41\n"
      "v_mul_f32 v255, v47, v255\n"
      "s_waitcnt lgkmcnt(6)\n"
      "buffer_store_dwordx4 v[188:191], v125, s[56:59], 0 offen offset:448 sc0 sc1 nt\n"
      "v_mfma_f32_16x16x32_bf16 v[36:39], v[80:83], a[112:115], v[36:39]\n"
      "v_exp_f32 v51, v51\n"
      "v_cvt_pk_bf16_f32 v248, v248, v249\n"
      "v_mfma_f32_16x16x32_bf16 v[36:39], v[88:91], a[120:123], v[36:39]\n"
      "ds_read_b64_tr_b16 v[80:81], v123 offset:0\n"
      "ds_read_b64_tr_b16 v[84:85], v123 offset:16384\n"
      "v_add_f32 v115, v115, v42\n"
      "v_cvt_pk_bf16_f32 v249, v250, v251\n"
      "v_cvt_pk_bf16_f32 v250, v252, v253\n"
      "v_mfma_f32_16x16x32_bf16 v[36:39], v[92:95], a[124:127], v[36:39]\n"
      "v_exp_f32 v4, v4\n"
      "v_cvt_pk_bf16_f32 v251, v254, v255\n"
      "v_mfma_f32_16x16x32_bf16 v[56:59], v[92:95], a[92:95], v[56:59]\n"
      "ds_read_b64_tr_b16 v[82:83], v123 offset:8192\n"
      "ds_read_b64_tr_b16 v[86:87], v123 offset:24576\n"
      "v_add_f32 v115, v115, v43\n"
      "ds_write_b64 v119, v[248:249] offset:7168\n"
      "ds_write_b64 v119, v[250:251] offset:7680\n"
      "v_mfma_f32_16x16x32_bf16 v[56:59], v[88:91], a[88:91], v[56:59]\n"
      "s_waitcnt lgkmcnt(10)\n"
      "v_exp_f32 v5, v5\n"
      "s_nop 0\n"
      "v_cvt_pk_bf16_f32 v35, v42, v43\n"
      "v_add_f32 v114, v114, v48\n"
      "v_add_f32 v114, v114, v49\n"
      "v_cvt_pk_bf16_f32 v48, v48, v49\n"
      "v_add_f32 v114, v114, v50\n"
      "v_add_f32 v114, v114, v51\n"
      "v_cvt_pk_bf16_f32 v49, v50, v51\n"
      "v_add_f32 v115, v115, v4\n"
      "v_add_f32 v115, v115, v5\n"
      "v_cvt_pk_bf16_f32 v4, v4, v5\n"
      "s_nop 7\n"
      "v_exp_f32 v6, v6\n"
      "v_exp_f32 v7, v7\n"
      "v_exp_f32 v56, v56\n"
      "v_exp_f32 v57, v57\n"
      "v_exp_f32 v58, v58\n"
      "v_exp_f32 v59, v59\n"
      "v_exp_f32 v36, v36\n"
      "v_exp_f32 v37, v37\n"
      "v_exp_f32 v38, v38\n"
      "v_exp_f32 v39, v39\n"
      "v_add_f32 v115, v115, v6\n"
      "v_add_f32 v115, v115, v7\n"
      "v_cvt_pk_bf16_f32 v5, v6, v7\n"
      "v_add_f32 v114, v114, v56\n"
      "v_add_f32 v114, v114, v57\n"
      "v_cvt_pk_bf16_f32 v50, v56, v57\n"
      "v_add_f32 v114, v114, v58\n"
      "v_add_f32 v114, v114, v59\n"
      "v_cvt_pk_bf16_f32 v51, v58, v59\n"
      "v_add_f32 v115, v115, v36\n"
      "v_add_f32 v115, v115, v37\n"
      "v_cvt_pk_bf16_f32 v6, v36, v37\n"
      "v_add_f32 v115, v115, v38\n"
      "v_add_f32 v115, v115, v39\n"
      "v_cvt_pk_bf16_f32 v7, v38, v39\n"
      "s_nop 0\n"
      "ds_read_b64_tr_b16 v[252:253], v120 offset:7168\n"
      "ds_read_b64_tr_b16 v[254:255], v121 offset:7168\n"
      "v_mfma_f32_16x16x32_bf16 a[128:131], v[16:19], v[64:67], a[128:131]\n"
      "ds_read_b64_tr_b16 v[88:89], v118 offset:0\n"
      "v_mov_b32 v100, v114\n"
      "v_mfma_f32_16x16x32_bf16 a[128:131], v[20:23], v[68:71], a[128:131]\n"
      "ds_read_b64_tr_b16 v[92:93], v118 offset:16384\n"
      "v_mov_b32 v101, v115\n"
      "s_nop 0\n"
      "v_mfma_f32_16x16x32_bf16 a[192:195], v[28:31], v[68:71], a[192:195]\n"
      "ds_read_b64_tr_b16 v[90:91], v118 offset:8192\n"
      "v_permlane32_swap_b32 v114, v100\n"
      "v_permlane32_swap_b32 v115, v101\n"
      "v_mfma_f32_16x16x32_bf16 a[192:195], v[24:27], v[64:67], a[192:195]\n"
      "s_waitcnt lgkmcnt(11)\n"
      "ds_read_b64_tr_b16 v[94:95], v118 offset:24576\n"
      "v_add_f32 v114, v114, v100\n"
      "v_add_f32 v115, v115, v101\n"
      "v_mfma_f32_16x16x32_bf16 a[192:195], v[32:35], v[72:75], a[192:195]\n"
      "ds_read_b64_tr_b16 v[64:65], v117 offset:512\n"
      "v_mov_b32 v100, v114\n"
      "v_mfma_f32_16x16x32_bf16 a[192:195], v[4:7], v[76:79], a[192:195]\n"
      "ds_read_b64_tr_b16 v[68:69], v117 offset:16896\n"
      "v_mov_b32 v101, v115\n"
      "s_nop 0\n"
      "v_mfma_f32_16x16x32_bf16 a[128:131], v[48:51], v[76:79], a[128:131]\n"
      "ds_read_b64_tr_b16 v[66:67], v117 offset:8704\n"
      "v_permlane16_swap_b32 v114, v100\n"
      "v_permlane16_swap_b32 v115, v101\n"
      "v_mfma_f32_16x16x32_bf16 a[128:131], v[0:3], v[72:75], a[128:131]\n"
      "s_waitcnt lgkmcnt(11)\n"
      "ds_read_b64_tr_b16 v[70:71], v117 offset:25088\n"
      "v_add_f32 v114, v114, v100\n"
      "v_add_f32 v115, v115, v101\n"
      "v_mfma_f32_16x16x32_bf16 a[132:135], v[0:3], v[80:83], a[132:135]\n"
      "ds_read_b64_tr_b16 v[72:73], v122 offset:512\n"
      "v_rcp_f32 v102, v114\n"
      "v_mfma_f32_16x16x32_bf16 a[132:135], v[48:51], v[84:87], a[132:135]\n"
      "ds_read_b64_tr_b16 v[76:77], v122 offset:16896\n"
      "v_rcp_f32 v103, v115\n"
      "v_cmp_nge_f32 vcc, s54, v114\n"
      "v_mfma_f32_16x16x32_bf16 a[196:199], v[4:7], v[84:87], a[196:199]\n"
      "ds_read_b64_tr_b16 v[74:75], v122 offset:8704\n"
      "s_or_b64 s[60:61], s[60:61], vcc\n"
      "v_cmp_nge_f32 vcc, s54, v115\n"
      "v_mfma_f32_16x16x32_bf16 a[196:199], v[32:35], v[80:83], a[196:199]\n"
      "s_waitcnt lgkmcnt(7)\n"
      "ds_read_b64_tr_b16 v[78:79], v122 offset:25088\n"
      "s_or_b64 s[60:61], s[60:61], vcc\n"
      "ds_bpermute_b32 v52, v124, v102 offset:0\n"
      "v_mfma_f32_16x16x32_bf16 a[196:199], v[24:27], v[88:91], a[196:199]\n"
      "ds_read_b64_tr_b16 v[80:81], v123 offset:512\n"
      "ds_bpermute_b32 v53, v124, v102 offset:4\n"
      "v_mfma_f32_16x16x32_bf16 a[196:199], v[28:31], v[92:95], a[196:199]\n"
      "ds_read_b64_tr_b16 v[84:85], v123 offset:16896\n"
      "ds_bpermute_b32 v54, v124, v102 offset:8\n"
      "ds_bpermute_b32 v55, v124, v102 offset:12\n"
      "v_mfma_f32_16x16x32_bf16 a[132:135], v[20:23], v[92:95], a[132:135]\n"
      "ds_read_b64_tr_b16 v[82:83], v123 offset:8704\n"
      "ds_bpermute_b32 v60, v124, v103 offset:0\n"
      "ds_bpermute_b32 v61, v124, v103 offset:4\n"
      "v_mfma_f32_16x16x32_bf16 a[132:135], v[16:19], v[88:91], a[132:135]\n"
      "s_waitcnt lgkmcnt(13)\n"
      "ds_read_b64_tr_b16 v[86:87], v123 offset:25088\n"
      "ds_bpermute_b32 v62, v124, v103 offset:8\n"
      "ds_bpermute_b32 v63, v124, v103 offset:12\n"
      "v_mfma_f32_16x16x32_bf16 a[136:139], v[16:19], v[64:67], a[136:139]\n"
      "ds_read_b64_tr_b16 v[88:89], v118 offset:512\n"
      "v_mfma_f32_16x16x32_bf16 a[136:139], v[20:23], v[68:71], a[136:139]\n"
      "ds_read_b64_tr_b16 v[92:93], v118 offset:16896\n"
      "v_mfma_f32_16x16x32_bf16 a[200:203], v[28:31], v[68:71], a[200:203]\n"
      "ds_read_b64_tr_b16 v[90:91], v118 offset:8704\n"
      "v_mfma_f32_16x16x32_bf16 a[200:203], v[24:27], v[64:67], a[200:203]\n"
      "s_waitcnt lgkmcnt(15)\n"
      "ds_read_b64_tr_b16 v[94:95], v118 offset:25088\n"
      "v_mfma_f32_16x16x32_bf16 a[200:203], v[32:35], v[72:75], a[200:203]\n"
      "ds_read_b64_tr_b16 v[64:65], v117 offset:1024\n"
      "v_accvgpr_read_b32 v128, a128\n"
      "v_accvgpr_read_b32 v129, a129\n"
      "v_mfma_f32_16x16x32_bf16 a[200:203], v[4:7], v[76:79], a[200:203]\n"
      "ds_read_b64_tr_b16 v[68:69], v117 offset:17408\n"
      "v_accvgpr_read_b32 v130, a130\n"
      "v_accvgpr_read_b32 v131, a131\n"
      "v_accvgpr_read_b32 v132, a132\n"
      "v_mfma_f32_16x16x32_bf16 a[136:139], v[48:51], v[76:79], a[136:139]\n"
      "ds_read_b64_tr_b16 v[66:67], v117 offset:9216\n"
      "v_accvgpr_read_b32 v133, a133\n"
      "v_accvgpr_read_b32 v134, a134\n"
      "v_accvgpr_read_b32 v135, a135\n"
      "v_mfma_f32_16x16x32_bf16 a[136:139], v[0:3], v[72:75], a[136:139]\n"
      "s_waitcnt lgkmcnt(9)\n"
      "ds_read_b64_tr_b16 v[70:71], v117 offset:25600\n"
      "v_mul_f32 v128, v52, v128\n"
      "v_mul_f32 v129, v53, v129\n"
      "v_mul_f32 v130, v54, v130\n"
      "v_mfma_f32_16x16x32_bf16 a[140:143], v[0:3], v[80:83], a[140:143]\n"
      "ds_read_b64_tr_b16 v[72:73], v122 offset:1024\n"
      "v_mul_f32 v131, v55, v131\n"
      "buffer_store_dwordx4 v[252:255], v96, s[56:59], 0 offen offset:448 sc0 sc1 nt\n"
      "v_mul_f32 v132, v52, v132\n"
      "v_mfma_f32_16x16x32_bf16 a[140:143], v[48:51], v[84:87], a[140:143]\n"
      "ds_read_b64_tr_b16 v[76:77], v122 offset:17408\n"
      "v_mul_f32 v133, v53, v133\n"
      "v_mul_f32 v134, v54, v134\n"
      "v_mul_f32 v135, v55, v135\n"
      "v_mfma_f32_16x16x32_bf16 a[204:207], v[4:7], v[84:87], a[204:207]\n"
      "ds_read_b64_tr_b16 v[74:75], v122 offset:9216\n"
      "v_cvt_pk_bf16_f32 v128, v128, v129\n"
      "v_cvt_pk_bf16_f32 v129, v130, v131\n"
      "v_cvt_pk_bf16_f32 v130, v132, v133\n"
      "v_mfma_f32_16x16x32_bf16 a[204:207], v[32:35], v[80:83], a[204:207]\n"
      "s_waitcnt lgkmcnt(7)\n"
      "ds_read_b64_tr_b16 v[78:79], v122 offset:25600\n"
      "v_cvt_pk_bf16_f32 v131, v134, v135\n"
      "ds_write_b64 v119, v[128:129] offset:0\n"
      "ds_write_b64 v119, v[130:131] offset:512\n"
      "v_mfma_f32_16x16x32_bf16 a[204:207], v[24:27], v[88:91], a[204:207]\n"
      "ds_read_b64_tr_b16 v[80:81], v123 offset:1024\n"
      "ds_read_b64_tr_b16 v[132:133], v120 offset:0\n"
      "ds_read_b64_tr_b16 v[134:135], v121 offset:0\n"
      "v_accvgpr_read_b32 v136, a192\n"
      "v_mfma_f32_16x16x32_bf16 a[204:207], v[28:31], v[92:95], a[204:207]\n"
      "ds_read_b64_tr_b16 v[84:85], v123 offset:17408\n"
      "v_accvgpr_read_b32 v137, a193\n"
      "v_accvgpr_read_b32 v138, a194\n"
      "v_mfma_f32_16x16x32_bf16 a[140:143], v[20:23], v[92:95], a[140:143]\n"
      "ds_read_b64_tr_b16 v[82:83], v123 offset:9216\n"
      "v_accvgpr_read_b32 v139, a195\n"
      "v_accvgpr_read_b32 v140, a196\n"
      "v_accvgpr_read_b32 v141, a197\n"
      "v_mfma_f32_16x16x32_bf16 a[140:143], v[16:19], v[88:91], a[140:143]\n"
      "s_waitcnt lgkmcnt(11)\n"
      "ds_read_b64_tr_b16 v[86:87], v123 offset:25600\n"
      "v_accvgpr_read_b32 v142, a198\n"
      "v_accvgpr_read_b32 v143, a199\n"
      "v_mul_f32 v136, v60, v136\n"
      "v_mfma_f32_16x16x32_bf16 a[144:147], v[16:19], v[64:67], a[144:147]\n"
      "ds_read_b64_tr_b16 v[88:89], v118 offset:1024\n"
      "v_mul_f32 v137, v61, v137\n"
      "v_mul_f32 v138, v62, v138\n"
      "v_mul_f32 v139, v63, v139\n"
      "v_mfma_f32_16x16x32_bf16 a[144:147], v[20:23], v[68:71], a[144:147]\n"
      "ds_read_b64_tr_b16 v[92:93], v118 offset:17408\n"
      "s_waitcnt lgkmcnt(5)\n"
      "buffer_store_dwordx4 v[132:135], v97, s[56:59], 0 offen offset:0 sc0 sc1 nt\n"
      "v_mul_f32 v140, v60, v140\n"
      "v_mul_f32 v141, v61, v141\n"
      "v_mfma_f32_16x16x32_bf16 a[208:211], v[28:31], v[68:71], a[208:211]\n"
      "ds_read_b64_tr_b16 v[90:91], v118 offset:9216\n"
      "v_mul_f32 v142, v62, v142\n"
      "v_mul_f32 v143, v63, v143\n"
      "v_cvt_pk_bf16_f32 v136, v136, v137\n"
      "v_mfma_f32_16x16x32_bf16 a[208:211], v[24:27], v[64:67], a[208:211]\n"
      "ds_read_b64_tr_b16 v[94:95], v118 offset:25600\n"
      "v_cvt_pk_bf16_f32 v137, v138, v139\n"
      "v_cvt_pk_bf16_f32 v138, v140, v141\n"
      "v_cvt_pk_bf16_f32 v139, v142, v143\n"
      "v_mfma_f32_16x16x32_bf16 a[208:211], v[32:35], v[72:75], a[208:211]\n"
      "ds_read_b64_tr_b16 v[64:65], v117 offset:1536\n"
      "ds_write_b64 v119, v[136:137] offset:1024\n"
      "ds_write_b64 v119, v[138:139] offset:1536\n"
      "ds_read_b64_tr_b16 v[140:141], v120 offset:1024\n"
      "v_mfma_f32_16x16x32_bf16 a[208:211], v[4:7], v[76:79], a[208:211]\n"
      "ds_read_b64_tr_b16 v[68:69], v117 offset:17920\n"
      "ds_read_b64_tr_b16 v[142:143], v121 offset:1024\n"
      "v_accvgpr_read_b32 v144, a136\n"
      "v_accvgpr_read_b32 v145, a137\n"
      "v_mfma_f32_16x16x32_bf16 a[144:147], v[48:51], v[76:79], a[144:147]\n"
      "ds_read_b64_tr_b16 v[66:67], v117 offset:9728\n"
      "v_accvgpr_read_b32 v146, a138\n"
      "v_accvgpr_read_b32 v147, a139\n"
      "v_accvgpr_read_b32 v148, a140\n"
      "v_mfma_f32_16x16x32_bf16 a[144:147], v[0:3], v[72:75], a[144:147]\n"
      "s_waitcnt lgkmcnt(11)\n"
      "ds_read_b64_tr_b16 v[70:71], v117 offset:26112\n"
      "v_accvgpr_read_b32 v149, a141\n"
      "v_accvgpr_read_b32 v150, a142\n"
      "v_mfma_f32_16x16x32_bf16 a[148:151], v[0:3], v[80:83], a[148:151]\n"
      "ds_read_b64_tr_b16 v[72:73], v122 offset:1536\n"
      "v_accvgpr_read_b32 v151, a143\n"
      "v_mul_f32 v144, v52, v144\n"
      "v_mul_f32 v145, v53, v145\n"
      "v_mfma_f32_16x16x32_bf16 a[148:151], v[48:51], v[84:87], a[148:151]\n"
      "ds_read_b64_tr_b16 v[76:77], v122 offset:17920\n"
      "v_mul_f32 v146, v54, v146\n"
      "v_mul_f32 v147, v55, v147\n"
      "s_waitcnt lgkmcnt(4)\n"
      "buffer_store_dwordx4 v[140:143], v98, s[56:59], 0 offen offset:0 sc0 sc1 nt\n"
      "v_mfma_f32_16x16x32_bf16 a[212:215], v[4:7], v[84:87], a[212:215]\n"
      "ds_read_b64_tr_b16 v[74:75], v122 offset:9728\n"
      "v_mul_f32 v148, v52, v148\n"
      "v_mul_f32 v149, v53, v149\n"
      "v_mul_f32 v150, v54, v150\n"
      "v_mfma_f32_16x16x32_bf16 a[212:215], v[32:35], v[80:83], a[212:215]\n"
      "ds_read_b64_tr_b16 v[78:79], v122 offset:26112\n"
      "v_mul_f32 v151, v55, v151\n"
      "v_cvt_pk_bf16_f32 v144, v144, v145\n"
      "v_cvt_pk_bf16_f32 v145, v146, v147\n"
      "v_mfma_f32_16x16x32_bf16 a[212:215], v[24:27], v[88:91], a[212:215]\n"
      "ds_read_b64_tr_b16 v[80:81], v123 offset:1536\n"
      "v_cvt_pk_bf16_f32 v146, v148, v149\n"
      "v_cvt_pk_bf16_f32 v147, v150, v151\n"
      "ds_write_b64 v119, v[144:145] offset:2048\n"
      "v_mfma_f32_16x16x32_bf16 a[212:215], v[28:31], v[92:95], a[212:215]\n"
      "ds_read_b64_tr_b16 v[84:85], v123 offset:17920\n"
      "ds_write_b64 v119, v[146:147] offset:2560\n"
      "ds_read_b64_tr_b16 v[148:149], v120 offset:2048\n"
      "ds_read_b64_tr_b16 v[150:151], v121 offset:2048\n"
      "v_mfma_f32_16x16x32_bf16 a[148:151], v[20:23], v[92:95], a[148:151]\n"
      "ds_read_b64_tr_b16 v[82:83], v123 offset:9728\n"
      "v_accvgpr_read_b32 v152, a200\n"
      "v_accvgpr_read_b32 v153, a201\n"
      "v_accvgpr_read_b32 v154, a202\n"
      "v_mfma_f32_16x16x32_bf16 a[148:151], v[16:19], v[88:91], a[148:151]\n"
      "s_waitcnt lgkmcnt(11)\n"
      "ds_read_b64_tr_b16 v[86:87], v123 offset:26112\n"
      "v_accvgpr_read_b32 v155, a203\n"
      "v_accvgpr_read_b32 v156, a204\n"
      "v_accvgpr_read_b32 v157, a205\n"
      "v_mfma_f32_16x16x32_bf16 a[152:155], v[16:19], v[64:67], a[152:155]\n"
      "ds_read_b64_tr_b16 v[88:89], v118 offset:1536\n"
      "v_accvgpr_read_b32 v158, a206\n"
      "v_accvgpr_read_b32 v159, a207\n"
      "v_mul_f32 v152, v60, v152\n"
      "v_mfma_f32_16x16x32_bf16 a[152:155], v[20:23], v[68:71], a[152:155]\n"
      "ds_read_b64_tr_b16 v[92:93], v118 offset:17920\n"
      "v_mul_f32 v153, v61, v153\n"
      "v_mul_f32 v154, v62, v154\n"
      "v_mfma_f32_16x16x32_bf16 a[216:219], v[28:31], v[68:71], a[216:219]\n"
      "ds_read_b64_tr_b16 v[90:91], v118 offset:9728\n"
      "v_mul_f32 v155, v63, v155\n"
      "s_waitcnt lgkmcnt(5)\n"
      "buffer_store_dwordx4 v[148:151], v97, s[56:59], 0 offen offset:64 sc0 sc1 nt\n"
      "v_mul_f32 v156, v60, v156\n"
      "v_mfma_f32_16x16x32_bf16 a[216:219], v[24:27], v[64:67], a[216:219]\n"
      "ds_read_b64_tr_b16 v[94:95], v118 offset:26112\n"
      "v_mul_f32 v157, v61, v157\n"
      "v_mul_f32 v158, v62, v158\n"
      "v_mul_f32 v159, v63, v159\n"
      "v_mfma_f32_16x16x32_bf16 a[216:219], v[32:35], v[72:75], a[216:219]\n"
      "ds_read_b64_tr_b16 v[64:65], v117 offset:2048\n"
      "v_cvt_pk_bf16_f32 v152, v152, v153\n"
      "v_cvt_pk_bf16_f32 v153, v154, v155\n"
      "v_cvt_pk_bf16_f32 v154, v156, v157\n"
      "v_mfma_f32_16x16x32_bf16 a[216:219], v[4:7], v[76:79], a[216:219]\n"
      "ds_read_b64_tr_b16 v[68:69], v117 offset:18432\n"
      "v_cvt_pk_bf16_f32 v155, v158, v159\n"
      "ds_write_b64 v119, v[152:153] offset:3072\n"
      "ds_write_b64 v119, v[154:155] offset:3584\n"
      "v_mfma_f32_16x16x32_bf16 a[152:155], v[48:51], v[76:79], a[152:155]\n"
      "ds_read_b64_tr_b16 v[66:67], v117 offset:10240\n"
      "ds_read_b64_tr_b16 v[156:157], v120 offset:3072\n"
      "ds_read_b64_tr_b16 v[158:159], v121 offset:3072\n"
      "v_accvgpr_read_b32 v128, a144\n"
      "v_mfma_f32_16x16x32_bf16 a[152:155], v[0:3], v[72:75], a[152:155]\n"
      "s_waitcnt lgkmcnt(11)\n"
      "ds_read_b64_tr_b16 v[70:71], v117 offset:26624\n"
      "v_accvgpr_read_b32 v129, a145\n"
      "v_accvgpr_read_b32 v130, a146\n"
      "v_accvgpr_read_b32 v131, a147\n"
      "v_mfma_f32_16x16x32_bf16 a[156:159], v[0:3], v[80:83], a[156:159]\n"
      "ds_read_b64_tr_b16 v[72:73], v122 offset:2048\n"
      "v_accvgpr_read_b32 v132, a148\n"
      "v_accvgpr_read_b32 v133, a149\n"
      "v_accvgpr_read_b32 v134, a150\n"
      "v_mfma_f32_16x16x32_bf16 a[156:159], v[48:51], v[84:87], a[156:159]\n"
      "ds_read_b64_tr_b16 v[76:77], v122 offset:18432\n"
      "v_accvgpr_read_b32 v135, a151\n"
      "v_mul_f32 v128, v52, v128\n"
      "v_mul_f32 v129, v53, v129\n"
      "v_mfma_f32_16x16x32_bf16 a[220:223], v[4:7], v[84:87], a[220:223]\n"
      "ds_read_b64_tr_b16 v[74:75], v122 offset:10240\n"
      "v_mul_f32 v130, v54, v130\n"
      "v_mul_f32 v131, v55, v131\n"
      "s_waitcnt lgkmcnt(4)\n"
      "buffer_store_dwordx4 v[156:159], v98, s[56:59], 0 offen offset:64 sc0 sc1 nt\n"
      "v_mfma_f32_16x16x32_bf16 a[220:223], v[32:35], v[80:83], a[220:223]\n"
      "ds_read_b64_tr_b16 v[78:79], v122 offset:26624\n"
      "v_mul_f32 v132, v52, v132\n"
      "v_mul_f32 v133, v53, v133\n"
      "v_mfma_f32_16x16x32_bf16 a[220:223], v[24:27], v[88:91], a[220:223]\n"
      "ds_read_b64_tr_b16 v[80:81], v123 offset:2048\n"
      "v_mul_f32 v134, v54, v134\n"
      "v_mul_f32 v135, v55, v135\n"
      "v_cvt_pk_bf16_f32 v128, v128, v129\n"
      "v_mfma_f32_16x16x32_bf16 a[220:223], v[28:31], v[92:95], a[220:223]\n"
      "ds_read_b64_tr_b16 v[84:85], v123 offset:18432\n"
      "v_cvt_pk_bf16_f32 v129, v130, v131\n"
      "v_cvt_pk_bf16_f32 v130, v132, v133\n"
      "v_cvt_pk_bf16_f32 v131, v134, v135\n"
      "v_mfma_f32_16x16x32_bf16 a[156:159], v[20:23], v[92:95], a[156:159]\n"
      "ds_read_b64_tr_b16 v[82:83], v123 offset:10240\n"
      "ds_write_b64 v119, v[128:129] offset:4096\n"
      "ds_write_b64 v119, v[130:131] offset:4608\n"
      "ds_read_b64_tr_b16 v[132:133], v120 offset:4096\n"
      "v_mfma_f32_16x16x32_bf16 a[156:159], v[16:19], v[88:91], a[156:159]\n"
      "s_waitcnt lgkmcnt(10)\n"
      "ds_read_b64_tr_b16 v[86:87], v123 offset:26624\n"
      "ds_read_b64_tr_b16 v[134:135], v121 offset:4096\n"
      "v_accvgpr_read_b32 v136, a208\n"
      "v_accvgpr_read_b32 v137, a209\n"
      "v_mfma_f32_16x16x32_bf16 a[160:163], v[16:19], v[64:67], a[160:163]\n"
      "ds_read_b64_tr_b16 v[88:89], v118 offset:2048\n"
      "v_accvgpr_read_b32 v138, a210\n"
      "v_accvgpr_read_b32 v139, a211\n"
      "v_accvgpr_read_b32 v140, a212\n"
      "v_mfma_f32_16x16x32_bf16 a[160:163], v[20:23], v[68:71], a[160:163]\n"
      "ds_read_b64_tr_b16 v[92:93], v118 offset:18432\n"
      "v_accvgpr_read_b32 v141, a213\n"
      "v_accvgpr_read_b32 v142, a214\n"
      "v_accvgpr_read_b32 v143, a215\n"
      "v_mfma_f32_16x16x32_bf16 a[224:227], v[28:31], v[68:71], a[224:227]\n"
      "ds_read_b64_tr_b16 v[90:91], v118 offset:10240\n"
      "v_mul_f32 v136, v60, v136\n"
      "v_mul_f32 v137, v61, v137\n"
      "v_mul_f32 v138, v62, v138\n"
      "v_mfma_f32_16x16x32_bf16 a[224:227], v[24:27], v[64:67], a[224:227]\n"
      "s_waitcnt lgkmcnt(11)\n"
      "ds_read_b64_tr_b16 v[94:95], v118 offset:26624\n"
      "v_mul_f32 v139, v63, v139\n"
      "s_waitcnt lgkmcnt(4)\n"
      "buffer_store_dwordx4 v[132:135], v97, s[56:59], 0 offen offset:128 sc0 sc1 nt\n"
      "v_mul_f32 v140, v60, v140\n"
      "v_mfma_f32_16x16x32_bf16 a[224:227], v[32:35], v[72:75], a[224:227]\n"
      "ds_read_b64_tr_b16 v[64:65], v117 offset:2560\n"
      "v_mul_f32 v141, v61, v141\n"
      "v_mul_f32 v142, v62, v142\n"
      "v_mul_f32 v143, v63, v143\n"
      "v_mfma_f32_16x16x32_bf16 a[224:227], v[4:7], v[76:79], a[224:227]\n"
      "ds_read_b64_tr_b16 v[68:69], v117 offset:18944\n"
      "v_cvt_pk_bf16_f32 v136, v136, v137\n"
      "v_cvt_pk_bf16_f32 v137, v138, v139\n"
      "v_mfma_f32_16x16x32_bf16 a[160:163], v[48:51], v[76:79], a[160:163]\n"
      "ds_read_b64_tr_b16 v[66:67], v117 offset:10752\n"
      "v_cvt_pk_bf16_f32 v138, v140, v141\n"
      "v_cvt_pk_bf16_f32 v139, v142, v143\n"
      "ds_write_b64 v119, v[136:137] offset:5120\n"
      "v_mfma_f32_16x16x32_bf16 a[160:163], v[0:3], v[72:75], a[160:163]\n"
      "ds_read_b64_tr_b16 v[70:71], v117 offset:27136\n"
      "ds_write_b64 v119, v[138:139] offset:5632\n"
      "ds_read_b64_tr_b16 v[140:141], v120 offset:5120\n"
      "ds_read_b64_tr_b16 v[142:143], v121 offset:5120\n"
      "v_mfma_f32_16x16x32_bf16 a[164:167], v[0:3], v[80:83], a[164:167]\n"
      "ds_read_b64_tr_b16 v[72:73], v122 offset:2560\n"
      "v_accvgpr_read_b32 v144, a152\n"
      "v_accvgpr_read_b32 v145, a153\n"
      "v_accvgpr_read_b32 v146, a154\n"
      "v_mfma_f32_16x16x32_bf16 a[164:167], v[48:51], v[84:87], a[164:167]\n"
      "ds_read_b64_tr_b16 v[76:77], v122 offset:18944\n"
      "v_accvgpr_read_b32 v147, a155\n"
      "v_accvgpr_read_b32 v148, a156\n"
      "v_accvgpr_read_b32 v149, a157\n"
      "v_mfma_f32_16x16x32_bf16 a[228:231], v[4:7], v[84:87], a[228:231]\n"
      "ds_read_b64_tr_b16 v[74:75], v122 offset:10752\n"
      "v_accvgpr_read_b32 v150, a158\n"
      "v_accvgpr_read_b32 v151, a159\n"
      "v_mul_f32 v144, v52, v144\n"
      "v_mfma_f32_16x16x32_bf16 a[228:231], v[32:35], v[80:83], a[228:231]\n"
      "s_waitcnt lgkmcnt(11)\n"
      "ds_read_b64_tr_b16 v[78:79], v122 offset:27136\n"
      "v_mul_f32 v145, v53, v145\n"
      "v_mul_f32 v146, v54, v146\n"
      "v_mul_f32 v147, v55, v147\n"
      "v_mfma_f32_16x16x32_bf16 a[228:231], v[24:27], v[88:91], a[228:231]\n"
      "ds_read_b64_tr_b16 v[80:81], v123 offset:2560\n"
      "s_waitcnt lgkmcnt(5)\n"
      "buffer_store_dwordx4 v[140:143], v98, s[56:59], 0 offen offset:128 sc0 sc1 nt\n"
      "v_mul_f32 v148, v52, v148\n"
      "v_mul_f32 v149, v53, v149\n"
      "v_mfma_f32_16x16x32_bf16 a[228:231], v[28:31], v[92:95], a[228:231]\n"
      "ds_read_b64_tr_b16 v[84:85], v123 offset:18944\n"
      "v_mul_f32 v150, v54, v150\n"
      "v_mul_f32 v151, v55, v151\n"
      "v_cvt_pk_bf16_f32 v144, v144, v145\n"
      "v_mfma_f32_16x16x32_bf16 a[164:167], v[20:23], v[92:95], a[164:167]\n"
      "ds_read_b64_tr_b16 v[82:83], v123 offset:10752\n"
      "v_cvt_pk_bf16_f32 v145, v146, v147\n"
      "v_cvt_pk_bf16_f32 v146, v148, v149\n"
      "v_mfma_f32_16x16x32_bf16 a[164:167], v[16:19], v[88:91], a[164:167]\n"
      "ds_read_b64_tr_b16 v[86:87], v123 offset:27136\n"
      "v_cvt_pk_bf16_f32 v147, v150, v151\n"
      "ds_write_b64 v119, v[144:145] offset:6144\n"
      "ds_write_b64 v119, v[146:147] offset:6656\n"
      "v_mfma_f32_16x16x32_bf16 a[168:171], v[16:19], v[64:67], a[168:171]\n"
      "ds_read_b64_tr_b16 v[88:89], v118 offset:2560\n"
      "ds_read_b64_tr_b16 v[148:149], v120 offset:6144\n"
      "ds_read_b64_tr_b16 v[150:151], v121 offset:6144\n"
      "v_accvgpr_read_b32 v152, a216\n"
      "v_mfma_f32_16x16x32_bf16 a[168:171], v[20:23], v[68:71], a[168:171]\n"
      "ds_read_b64_tr_b16 v[92:93], v118 offset:18944\n"
      "v_accvgpr_read_b32 v153, a217\n"
      "v_accvgpr_read_b32 v154, a218\n"
      "v_accvgpr_read_b32 v155, a219\n"
      "v_mfma_f32_16x16x32_bf16 a[232:235], v[28:31], v[68:71], a[232:235]\n"
      "ds_read_b64_tr_b16 v[90:91], v118 offset:10752\n"
      "v_accvgpr_read_b32 v156, a220\n"
      "v_accvgpr_read_b32 v157, a221\n"
      "v_accvgpr_read_b32 v158, a222\n"
      "v_mfma_f32_16x16x32_bf16 a[232:235], v[24:27], v[64:67], a[232:235]\n"
      "s_waitcnt lgkmcnt(11)\n"
      "ds_read_b64_tr_b16 v[94:95], v118 offset:27136\n"
      "v_accvgpr_read_b32 v159, a223\n"
      "v_mul_f32 v152, v60, v152\n"
      "v_mul_f32 v153, v61, v153\n"
      "v_mfma_f32_16x16x32_bf16 a[232:235], v[32:35], v[72:75], a[232:235]\n"
      "ds_read_b64_tr_b16 v[64:65], v117 offset:3072\n"
      "v_mul_f32 v154, v62, v154\n"
      "v_mul_f32 v155, v63, v155\n"
      "s_waitcnt lgkmcnt(4)\n"
      "buffer_store_dwordx4 v[148:151], v97, s[56:59], 0 offen offset:192 sc0 sc1 nt\n"
      "v_mfma_f32_16x16x32_bf16 a[232:235], v[4:7], v[76:79], a[232:235]\n"
      "ds_read_b64_tr_b16 v[68:69], v117 offset:19456\n"
      "v_mul_f32 v156, v60, v156\n"
      "v_mul_f32 v157, v61, v157\n"
      "v_mul_f32 v158, v62, v158\n"
      "v_mfma_f32_16x16x32_bf16 a[168:171], v[48:51], v[76:79], a[168:171]\n"
      "ds_read_b64_tr_b16 v[66:67], v117 offset:11264\n"
      "v_mul_f32 v159, v63, v159\n"
      "v_cvt_pk_bf16_f32 v152, v152, v153\n"
      "v_cvt_pk_bf16_f32 v153, v154, v155\n"
      "v_mfma_f32_16x16x32_bf16 a[168:171], v[0:3], v[72:75], a[168:171]\n"
      "ds_read_b64_tr_b16 v[70:71], v117 offset:27648\n"
      "v_cvt_pk_bf16_f32 v154, v156, v157\n"
      "v_cvt_pk_bf16_f32 v155, v158, v159\n"
      "ds_write_b64 v119, v[152:153] offset:7168\n"
      "v_mfma_f32_16x16x32_bf16 a[172:175], v[0:3], v[80:83], a[172:175]\n"
      "ds_read_b64_tr_b16 v[72:73], v122 offset:3072\n"
      "ds_write_b64 v119, v[154:155] offset:7680\n"
      "ds_read_b64_tr_b16 v[156:157], v120 offset:7168\n"
      "v_mfma_f32_16x16x32_bf16 a[172:175], v[48:51], v[84:87], a[172:175]\n"
      "ds_read_b64_tr_b16 v[76:77], v122 offset:19456\n"
      "ds_read_b64_tr_b16 v[158:159], v121 offset:7168\n"
      "v_accvgpr_read_b32 v128, a160\n"
      "v_accvgpr_read_b32 v129, a161\n"
      "v_mfma_f32_16x16x32_bf16 a[236:239], v[4:7], v[84:87], a[236:239]\n"
      "ds_read_b64_tr_b16 v[74:75], v122 offset:11264\n"
      "v_accvgpr_read_b32 v130, a162\n"
      "v_accvgpr_read_b32 v131, a163\n"
      "v_accvgpr_read_b32 v132, a164\n"
      "v_mfma_f32_16x16x32_bf16 a[236:239], v[32:35], v[80:83], a[236:239]\n"
      "s_waitcnt lgkmcnt(11)\n"
      "ds_read_b64_tr_b16 v[78:79], v122 offset:27648\n"
      "v_accvgpr_read_b32 v133, a165\n"
      "v_accvgpr_read_b32 v134, a166\n"
      "v_accvgpr_read_b32 v135, a167\n"
      "v_mfma_f32_16x16x32_bf16 a[236:239], v[24:27], v[88:91], a[236:239]\n"
      "ds_read_b64_tr_b16 v[80:81], v123 offset:3072\n"
      "v_mul_f32 v128, v52, v128\n"
      "v_mul_f32 v129, v53, v129\n"
      "v_mul_f32 v130, v54, v130\n"
      "v_mfma_f32_16x16x32_bf16 a[236:239], v[28:31], v[92:95], a[236:239]\n"
      "ds_read_b64_tr_b16 v[84:85], v123 offset:19456\n"
      "v_mul_f32 v131, v55, v131\n"
      "s_waitcnt lgkmcnt(4)\n"
      "buffer_store_dwordx4 v[156:159], v98, s[56:59], 0 offen offset:192 sc0 sc1 nt\n"
      "v_mul_f32 v132, v52, v132\n"
      "v_mfma_f32_16x16x32_bf16 a[172:175], v[20:23], v[92:95], a[172:175]\n"
      "ds_read_b64_tr_b16 v[82:83], v123 offset:11264\n"
      "v_mul_f32 v133, v53, v133\n"
      "v_mul_f32 v134, v54, v134\n"
      "v_mul_f32 v135, v55, v135\n"
      "v_mfma_f32_16x16x32_bf16 a[172:175], v[16:19], v[88:91], a[172:175]\n"
      "ds_read_b64_tr_b16 v[86:87], v123 offset:27648\n"
      "v_cvt_pk_bf16_f32 v128, v128, v129\n"
      "v_cvt_pk_bf16_f32 v129, v130, v131\n"
      "v_cvt_pk_bf16_f32 v130, v132, v133\n"
      "v_mfma_f32_16x16x32_bf16 a[176:179], v[16:19], v[64:67], a[176:179]\n"
      "ds_read_b64_tr_b16 v[88:89], v118 offset:3072\n"
      "v_cvt_pk_bf16_f32 v131, v134, v135\n"
      "ds_write_b64 v119, v[128:129] offset:0\n"
      "ds_write_b64 v119, v[130:131] offset:512\n"
      "v_mfma_f32_16x16x32_bf16 a[176:179], v[20:23], v[68:71], a[176:179]\n"
      "ds_read_b64_tr_b16 v[92:93], v118 offset:19456\n"
      "ds_read_b64_tr_b16 v[132:133], v120 offset:0\n"
      "ds_read_b64_tr_b16 v[134:135], v121 offset:0\n"
      "v_accvgpr_read_b32 v136, a224\n"
      "v_mfma_f32_16x16x32_bf16 a[240:243], v[28:31], v[68:71], a[240:243]\n"
      "ds_read_b64_tr_b16 v[90:91], v118 offset:11264\n"
      "v_accvgpr_read_b32 v137, a225\n"
      "v_accvgpr_read_b32 v138, a226\n"
      "v_mfma_f32_16x16x32_bf16 a[240:243], v[24:27], v[64:67], a[240:243]\n"
      "s_waitcnt lgkmcnt(11)\n"
      "ds_read_b64_tr_b16 v[94:95], v118 offset:27648\n"
      "v_accvgpr_read_b32 v139, a227\n"
      "v_accvgpr_read_b32 v140, a228\n"
      "v_accvgpr_read_b32 v141, a229\n"
      "v_mfma_f32_16x16x32_bf16 a[240:243], v[32:35], v[72:75], a[240:243]\n"
      "ds_read_b64_tr_b16 v[64:65], v117 offset:3584\n"
      "v_accvgpr_read_b32 v142, a230\n"
      "v_accvgpr_read_b32 v143, a231\n"
      "v_mul_f32 v136, v60, v136\n"
      "v_mfma_f32_16x16x32_bf16 a[240:243], v[4:7], v[76:79], a[240:243]\n"
      "ds_read_b64_tr_b16 v[68:69], v117 offset:19968\n"
      "v_mul_f32 v137, v61, v137\n"
      "v_mul_f32 v138, v62, v138\n"
      "v_mul_f32 v139, v63, v139\n"
      "v_mfma_f32_16x16x32_bf16 a[176:179], v[48:51], v[76:79], a[176:179]\n"
      "ds_read_b64_tr_b16 v[66:67], v117 offset:11776\n"
      "s_waitcnt lgkmcnt(5)\n"
      "buffer_store_dwordx4 v[132:135], v97, s[56:59], 0 offen offset:256 sc0 sc1 nt\n"
      "v_mul_f32 v140, v60, v140\n"
      "v_mul_f32 v141, v61, v141\n"
      "v_mfma_f32_16x16x32_bf16 a[176:179], v[0:3], v[72:75], a[176:179]\n"
      "ds_read_b64_tr_b16 v[70:71], v117 offset:28160\n"
      "v_mul_f32 v142, v62, v142\n"
      "v_mul_f32 v143, v63, v143\n"
      "v_cvt_pk_bf16_f32 v136, v136, v137\n"
      "v_mfma_f32_16x16x32_bf16 a[180:183], v[0:3], v[80:83], a[180:183]\n"
      "ds_read_b64_tr_b16 v[72:73], v122 offset:3584\n"
      "v_cvt_pk_bf16_f32 v137, v138, v139\n"
      "v_cvt_pk_bf16_f32 v138, v140, v141\n"
      "v_cvt_pk_bf16_f32 v139, v142, v143\n"
      "v_mfma_f32_16x16x32_bf16 a[180:183], v[48:51], v[84:87], a[180:183]\n"
      "ds_read_b64_tr_b16 v[76:77], v122 offset:19968\n"
      "ds_write_b64 v119, v[136:137] offset:1024\n"
      "ds_write_b64 v119, v[138:139] offset:1536\n"
      "ds_read_b64_tr_b16 v[140:141], v120 offset:1024\n"
      "v_mfma_f32_16x16x32_bf16 a[244:247], v[4:7], v[84:87], a[244:247]\n"
      "ds_read_b64_tr_b16 v[74:75], v122 offset:11776\n"
      "ds_read_b64_tr_b16 v[142:143], v121 offset:1024\n"
      "v_accvgpr_read_b32 v144, a168\n"
      "v_accvgpr_read_b32 v145, a169\n"
      "v_mfma_f32_16x16x32_bf16 a[244:247], v[32:35], v[80:83], a[244:247]\n"
      "s_waitcnt lgkmcnt(11)\n"
      "ds_read_b64_tr_b16 v[78:79], v122 offset:28160\n"
      "v_accvgpr_read_b32 v146, a170\n"
      "v_accvgpr_read_b32 v147, a171\n"
      "v_accvgpr_read_b32 v148, a172\n"
      "v_mfma_f32_16x16x32_bf16 a[244:247], v[24:27], v[88:91], a[244:247]\n"
      "ds_read_b64_tr_b16 v[80:81], v123 offset:3584\n"
      "v_accvgpr_read_b32 v149, a173\n"
      "v_accvgpr_read_b32 v150, a174\n"
      "v_mfma_f32_16x16x32_bf16 a[244:247], v[28:31], v[92:95], a[244:247]\n"
      "ds_read_b64_tr_b16 v[84:85], v123 offset:19968\n"
      "v_accvgpr_read_b32 v151, a175\n"
      "v_mul_f32 v144, v52, v144\n"
      "v_mul_f32 v145, v53, v145\n"
      "v_mfma_f32_16x16x32_bf16 a[180:183], v[20:23], v[92:95], a[180:183]\n"
      "ds_read_b64_tr_b16 v[82:83], v123 offset:11776\n"
      "v_mul_f32 v146, v54, v146\n"
      "v_mul_f32 v147, v55, v147\n"
      "s_waitcnt lgkmcnt(4)\n"
      "buffer_store_dwordx4 v[140:143], v98, s[56:59], 0 offen offset:256 sc0 sc1 nt\n"
      "v_mfma_f32_16x16x32_bf16 a[180:183], v[16:19], v[88:91], a[180:183]\n"
      "ds_read_b64_tr_b16 v[86:87], v123 offset:28160\n"
      "v_mul_f32 v148, v52, v148\n"
      "v_mul_f32 v149, v53, v149\n"
      "v_mul_f32 v150, v54, v150\n"
      "v_mfma_f32_16x16x32_bf16 a[184:187], v[16:19], v[64:67], a[184:187]\n"
      "ds_read_b64_tr_b16 v[88:89], v118 offset:3584\n"
      "v_mul_f32 v151, v55, v151\n"
      "v_cvt_pk_bf16_f32 v144, v144, v145\n"
      "v_cvt_pk_bf16_f32 v145, v146, v147\n"
      "v_mfma_f32_16x16x32_bf16 a[184:187], v[20:23], v[68:71], a[184:187]\n"
      "ds_read_b64_tr_b16 v[92:93], v118 offset:19968\n"
      "v_cvt_pk_bf16_f32 v146, v148, v149\n"
      "v_cvt_pk_bf16_f32 v147, v150, v151\n"
      "ds_write_b64 v119, v[144:145] offset:2048\n"
      "v_mfma_f32_16x16x32_bf16 a[248:251], v[28:31], v[68:71], a[248:251]\n"
      "ds_read_b64_tr_b16 v[90:91], v118 offset:11776\n"
      "ds_write_b64 v119, v[146:147] offset:2560\n"
      "ds_read_b64_tr_b16 v[148:149], v120 offset:2048\n"
      "ds_read_b64_tr_b16 v[150:151], v121 offset:2048\n"
      "v_mfma_f32_16x16x32_bf16 a[248:251], v[24:27], v[64:67], a[248:251]\n"
      "s_waitcnt lgkmcnt(11)\n"
      "ds_read_b64_tr_b16 v[94:95], v118 offset:28160\n"
      "v_accvgpr_read_b32 v152, a232\n"
      "v_accvgpr_read_b32 v153, a233\n"
      "v_accvgpr_read_b32 v154, a234\n"
      "v_mfma_f32_16x16x32_bf16 a[248:251], v[32:35], v[72:75], a[248:251]\n"
      "v_accvgpr_read_b32 v155, a235\n"
      "v_accvgpr_read_b32 v156, a236\n"
      "v_accvgpr_read_b32 v157, a237\n"
      "v_accvgpr_read_b32 v158, a238\n"
      "v_mfma_f32_16x16x32_bf16 a[248:251], v[4:7], v[76:79], a[248:251]\n"
      "v_accvgpr_read_b32 v159, a239\n"
      "v_mul_f32 v152, v60, v152\n"
      "v_mul_f32 v153, v61, v153\n"
      "v_mul_f32 v154, v62, v154\n"
      "v_mfma_f32_16x16x32_bf16 a[184:187], v[48:51], v[76:79], a[184:187]\n"
      "v_mul_f32 v155, v63, v155\n"
      "s_waitcnt lgkmcnt(1)\n"
      "buffer_store_dwordx4 v[148:151], v97, s[56:59], 0 offen offset:320 sc0 sc1 nt\n"
      "v_mul_f32 v156, v60, v156\n"
      "v_mfma_f32_16x16x32_bf16 a[184:187], v[0:3], v[72:75], a[184:187]\n"
      "v_mul_f32 v157, v61, v157\n"
      "v_mul_f32 v158, v62, v158\n"
      "v_mul_f32 v159, v63, v159\n"
      "v_cvt_pk_bf16_f32 v152, v152, v153\n"
      "v_mfma_f32_16x16x32_bf16 a[188:191], v[0:3], v[80:83], a[188:191]\n"
      "v_cvt_pk_bf16_f32 v153, v154, v155\n"
      "v_cvt_pk_bf16_f32 v154, v156, v157\n"
      "v_cvt_pk_bf16_f32 v155, v158, v159\n"
      "ds_write_b64 v119, v[152:153] offset:3072\n"
      "v_mfma_f32_16x16x32_bf16 a[188:191], v[48:51], v[84:87], a[188:191]\n"
      "ds_write_b64 v119, v[154:155] offset:3584\n"
      "ds_read_b64_tr_b16 v[156:157], v120 offset:3072\n"
      "ds_read_b64_tr_b16 v[158:159], v121 offset:3072\n"
      "v_accvgpr_read_b32 v128, a176\n"
      "v_mfma_f32_16x16x32_bf16 a[252:255], v[4:7], v[84:87], a[252:255]\n"
      "v_accvgpr_read_b32 v129, a177\n"
      "v_accvgpr_read_b32 v130, a178\n"
      "v_accvgpr_read_b32 v131, a179\n"
      "v_accvgpr_read_b32 v132, a180\n"
      "v_mfma_f32_16x16x32_bf16 a[252:255], v[32:35], v[80:83], a[252:255]\n"
      "s_waitcnt lgkmcnt(4)\n"
      "v_accvgpr_read_b32 v133, a181\n"
      "v_accvgpr_read_b32 v134, a182\n"
      "v_accvgpr_read_b32 v135, a183\n"
      "v_mul_f32 v128, v52, v128\n"
      "v_mfma_f32_16x16x32_bf16 a[252:255], v[24:27], v[88:91], a[252:255]\n"
      "v_mul_f32 v129, v53, v129\n"
      "v_mul_f32 v130, v54, v130\n"
      "v_mul_f32 v131, v55, v131\n"
      "s_waitcnt lgkmcnt(0)\n"
      "buffer_store_dwordx4 v[156:159], v98, s[56:59], 0 offen offset:320 sc0 sc1 nt\n"
      "v_mfma_f32_16x16x32_bf16 a[252:255], v[28:31], v[92:95], a[252:255]\n"
      "v_mul_f32 v132, v52, v132\n"
      "v_mul_f32 v133, v53, v133\n"
      "v_mul_f32 v134, v54, v134\n"
      "v_mul_f32 v135, v55, v135\n"
      "v_mfma_f32_16x16x32_bf16 a[188:191], v[20:23], v[92:95], a[188:191]\n"
      "v_cvt_pk_bf16_f32 v128, v128, v129\n"
      "v_cvt_pk_bf16_f32 v129, v130, v131\n"
      "v_cvt_pk_bf16_f32 v130, v132, v133\n"
      "v_cvt_pk_bf16_f32 v131, v134, v135\n"
      "v_mfma_f32_16x16x32_bf16 a[188:191], v[16:19], v[88:91], a[188:191]\n"
      "ds_write_b64 v119, v[128:129] offset:4096\n"
      "ds_write_b64 v119, v[130:131] offset:4608\n"
      "ds_read_b64_tr_b16 v[132:133], v120 offset:4096\n"
      "ds_read_b64_tr_b16 v[134:135], v121 offset:4096\n"
      "v_accvgpr_read_b32 v136, a240\n"
      "v_accvgpr_read_b32 v137, a241\n"
      "v_accvgpr_read_b32 v138, a242\n"
      "v_accvgpr_read_b32 v139, a243\n"
      "v_accvgpr_read_b32 v140, a244\n"
      "v_accvgpr_read_b32 v141, a245\n"
      "v_accvgpr_read_b32 v142, a246\n"
      "v_accvgpr_read_b32 v143, a247\n"
      "v_mul_f32 v136, v60, v136\n"
      "v_mul_f32 v137, v61, v137\n"
      "v_mul_f32 v138, v62, v138\n"
      "v_mul_f32 v139, v63, v139\n"
      "s_waitcnt lgkmcnt(0)\n"
      "buffer_store_dwordx4 v[132:135], v97, s[56:59], 0 offen offset:384 sc0 sc1 nt\n"
      "v_mul_f32 v140, v60, v140\n"
      "v_mul_f32 v141, v61, v141\n"
      "v_mul_f32 v142, v62, v142\n"
      "v_mul_f32 v143, v63, v143\n"
      "v_cvt_pk_bf16_f32 v136, v136, v137\n"
      "v_cvt_pk_bf16_f32 v137, v138, v139\n"
      "v_cvt_pk_bf16_f32 v138, v140, v141\n"
      "v_cvt_pk_bf16_f32 v139, v142, v143\n"
      "ds_write_b64 v119, v[136:137] offset:5120\n"
      "ds_write_b64 v119, v[138:139] offset:5632\n"
      "ds_read_b64_tr_b16 v[140:141], v120 offset:5120\n"
      "ds_read_b64_tr_b16 v[142:143], v121 offset:5120\n"
      "v_accvgpr_read_b32 v144, a184\n"
      "v_accvgpr_read_b32 v145, a185\n"
      "v_accvgpr_read_b32 v146, a186\n"
      "v_accvgpr_read_b32 v147, a187\n"
      "v_accvgpr_read_b32 v148, a188\n"
      "v_accvgpr_read_b32 v149, a189\n"
      "v_accvgpr_read_b32 v150, a190\n"
      "v_accvgpr_read_b32 v151, a191\n"
      "v_mul_f32 v144, v52, v144\n"
      "v_mul_f32 v145, v53, v145\n"
      "v_mul_f32 v146, v54, v146\n"
      "v_mul_f32 v147, v55, v147\n"
      "s_waitcnt lgkmcnt(0)\n"
      "buffer_store_dwordx4 v[140:143], v98, s[56:59], 0 offen offset:384 sc0 sc1 nt\n"
      "v_mul_f32 v148, v52, v148\n"
      "v_mul_f32 v149, v53, v149\n"
      "v_mul_f32 v150, v54, v150\n"
      "v_mul_f32 v151, v55, v151\n"
      "v_cvt_pk_bf16_f32 v144, v144, v145\n"
      "v_cvt_pk_bf16_f32 v145, v146, v147\n"
      "v_cvt_pk_bf16_f32 v146, v148, v149\n"
      "v_cvt_pk_bf16_f32 v147, v150, v151\n"
      "ds_write_b64 v119, v[144:145] offset:6144\n"
      "ds_write_b64 v119, v[146:147] offset:6656\n"
      "ds_read_b64_tr_b16 v[148:149], v120 offset:6144\n"
      "ds_read_b64_tr_b16 v[150:151], v121 offset:6144\n"
      "v_accvgpr_read_b32 v152, a248\n"
      "v_accvgpr_read_b32 v153, a249\n"
      "v_accvgpr_read_b32 v154, a250\n"
      "v_accvgpr_read_b32 v155, a251\n"
      "v_accvgpr_read_b32 v156, a252\n"
      "v_accvgpr_read_b32 v157, a253\n"
      "v_accvgpr_read_b32 v158, a254\n"
      "v_accvgpr_read_b32 v159, a255\n"
      "v_mul_f32 v152, v60, v152\n"
      "v_mul_f32 v153, v61, v153\n"
      "v_mul_f32 v154, v62, v154\n"
      "v_mul_f32 v155, v63, v155\n"
      "s_waitcnt lgkmcnt(0)\n"
      "buffer_store_dwordx4 v[148:151], v97, s[56:59], 0 offen offset:448 sc0 sc1 nt\n"
      "v_mul_f32 v156, v60, v156\n"
      "v_mul_f32 v157, v61, v157\n"
      "v_mul_f32 v158, v62, v158\n"
      "v_mul_f32 v159, v63, v159\n"
      "v_cvt_pk_bf16_f32 v152, v152, v153\n"
      "v_cvt_pk_bf16_f32 v153, v154, v155\n"
      "v_cvt_pk_bf16_f32 v154, v156, v157\n"
      "v_cvt_pk_bf16_f32 v155, v158, v159\n"
      "ds_write_b64 v119, v[152:153] offset:7168\n"
      "ds_write_b64 v119, v[154:155] offset:7680\n"
      "ds_read_b64_tr_b16 v[156:157], v120 offset:7168\n"
      "ds_read_b64_tr_b16 v[158:159], v121 offset:7168\n"
      "s_waitcnt lgkmcnt(0)\n"
      "buffer_store_dwordx4 v[156:159], v98, s[56:59], 0 offen offset:448 sc0 sc1 nt\n"
      "s_cmp_lg_u64 s[60:61], 0\n"
      "s_cselect_b32 %[flag], 1, 0\n"
      : [flag] "=s"(flag)
      : "s"(kargs), "s"(blk), "s"(wv)
      : "v0","v1","v2","v3","v4","v5","v6","v7","v8","v9","v10","v11","v12","v13","v14","v15","v16","v17","v18","v19","v20","v21","v22","v23","v24","v25","v26","v27","v28","v29","v30","v31","v32","v33","v34","v35","v36","v37","v38","v39","v40","v41","v42","v43","v44","v45","v46","v47","v48","v49","v50","v51","v52","v53","v54","v55","v56","v57","v58","v59","v60","v61","v62","v63","v64","v65","v66","v67","v68","v69","v70","v71","v72","v73","v74","v75","v76","v77","v78","v79","v80","v81","v82","v83","v84","v85","v86","v87","v88","v89","v90","v91","v92","v93","v94","v95","v96","v97","v98","v99","v100","v101","v102","v103","v104","v105","v106","v107","v108","v109","v110","v111","v112","v113","v114","v115","v116","v117","v118","v119","v120","v121","v122","v123","v124","v125","v126","v127","v128","v129","v130","v131","v132","v133","v134","v135","v136","v137","v138","v139","v140","v141","v142","v143","v144","v145","v146","v147","v148","v149","v150","v151","v152","v153","v154","v155","v156","v157","v158","v159","v160","v161","v162","v163","v164","v165","v166","v167","v168","v169","v170","v171","v172","v173","v174","v175","v176","v177","v178","v179","v180","v181","v182","v183","v184","v185","v186","v187","v188","v189","v190","v191","v192","v193","v194","v195","v196","v197","v198","v199","v200","v201","v202","v203","v204","v205","v206","v207","v208","v209","v210","v211","v212","v213","v214","v215","v216","v217","v218","v219","v220","v221","v222","v223","v224","v225","v226","v227","v228","v229","v230","v231","v232","v233","v234","v235","v236","v237","v238","v239","v240","v241","v242","v243","v244","v245","v246","v247","v248","v249","v250","v251","v252","v253","v254","v255","a0","a1","a2","a3","a4","a5","a6","a7","a8","a9","a10","a11","a12","a13","a14","a15","a16","a17","a18","a19","a20","a21","a22","a23","a24","a25","a26","a27","a28","a29","a30","a31","a32","a33","a34","a35","a36","a37","a38","a39","a40","a41","a42","a43","a44","a45","a46","a47","a48","a49","a50","a51","a52","a53","a54","a55","a56","a57","a58","a59","a60","a61","a62","a63","a64","a65","a66","a67","a68","a69","a70","a71","a72","a73","a74","a75","a76","a77","a78","a79","a80","a81","a82","a83","a84","a85","a86","a87","a88","a89","a90","a91","a92","a93","a94","a95","a96","a97","a98","a99","a100","a101","a102","a103","a104","a105","a106","a107","a108","a109","a110","a111","a112","a113","a114","a115","a116","a117","a118","a119","a120","a121","a122","a123","a124","a125","a126","a127","a128","a129","a130","a131","a132","a133","a134","a135","a136","a137","a138","a139","a140","a141","a142","a143","a144","a145","a146","a147","a148","a149","a150","a151","a152","a153","a154","a155","a156","a157","a158","a159","a160","a161","a162","a163","a164","a165","a166","a167","a168","a169","a170","a171","a172","a173","a174","a175","a176","a177","a178","a179","a180","a181","a182","a183","a184","a185","a186","a187","a188","a189","a190","a191","a192","a193","a194","a195","a196","a197","a198","a199","a200","a201","a202","a203","a204","a205","a206","a207","a208","a209","a210","a211","a212","a213","a214","a215","a216","a217","a218","a219","a220","a221","a222","a223","a224","a225","a226","a227","a228","a229","a230","a231","a232","a233","a234","a235","a236","a237","a238","a239","a240","a241","a242","a243","a244","a245","a246","a247","a248","a249","a250","a251","a252","a253","a254","a255","s20","s21","s22","s23","s24","s25","s26","s27","s28","s29","s30","s31","s36","s37","s38","s39","s40","s41","s42","s43","s44","s45","s46","s47","s48","s49","s50","s51","s52","s53","s54","s55","s56","s57","s58","s59","s60","s61","s62","s63","s64","s65","s66","s67","s68","s69","s70","s71","s72","s73","s74","s75","s76","s77","s78","s79","s80","s81","s82","s83","s84","s85","s86","s87","s88","s89","s90","s91","s92","s93","s94","vcc","scc","memory");
  if (flag) {
    asm volatile("s_waitcnt vmcnt(0)" ::: "memory");
    const unsigned lane = lane_id();
    const unsigned head = (blk & 7u) + 8u * (blk >> 4), row0 = ((blk >> 3) & 1u) * 256u + wv * 64u;
    float* sc = (float*)(lds + 98304 + wv * 8192);
    const unsigned short* Kh = K + (size_t)head * SK * HD;
    const unsigned short* Vh = V + (size_t)head * SK * HD;
#pragma unroll 1
    for (unsigned r = row0; r < row0 + 64u && r < (unsigned)SQ; ++r) {
      const unsigned short* q = Q + ((size_t)head * SQ + r) * HD;
      float mx = -INFINITY;
#pragma unroll 1
      for (unsigned j = lane; j < (unsigned)SK; j += 64u) {
        float a = 0.f;
#pragma unroll 2
        for (unsigned d = 0; d < (unsigned)HD; d += 8u) {
          const u32x4 kk = *(const u32x4*)(Kh + (size_t)j * HD + d);
          const u32x4 qq = *(const u32x4*)(q + d);
#pragma unroll
          for (int i = 0; i < 4; ++i) { a = fmaf(bf_lo(kk[i]), bf_lo(qq[i]), a); a = fmaf(bf_hi(kk[i]), bf_hi(qq[i]), a); }
        }
        a *= 0.0625f;
        sc[j] = a;
        mx = fmaxf(mx, a);
      }
#pragma unroll
      for (unsigned o = 32; o > 0; o >>= 1) mx = fmaxf(mx, bperm_f(lane ^ o, mx));
      float sum = 0.f;
#pragma unroll 1
      for (unsigned j = lane; j < (unsigned)SK; j += 64u) { const float p = __expf(sc[j] - mx); sc[j] = p; sum += p; }
#pragma unroll
      for (unsigned o = 32; o > 0; o >>= 1) sum += bperm_f(lane ^ o, sum);
      __builtin_amdgcn_fence(__ATOMIC_SEQ_CST, "wavefront");
      float a0 = 0.f, a1 = 0.f, a2 = 0.f, a3 = 0.f;
#pragma unroll 4
      for (unsigned j = 0; j < (unsigned)SK; ++j) {
        const float p = sc[j];
        const u32x2 vv = *(const u32x2*)(Vh + (size_t)j * HD + lane * 4u);
        a0 = fmaf(p, bf_lo(vv[0]), a0); a1 = fmaf(p, bf_hi(vv[0]), a1); a2 = fmaf(p, bf_lo(vv[1]), a2); a3 = fmaf(p, bf_hi(vv[1]), a3);
      }
      const float inv = 1.0f / sum;
      __hip_bfloat16* o = (__hip_bfloat16*)(O + ((size_t)head * SQ + r) * HD + lane * 4u);
      o[0] = __float2bfloat16(a0 * inv); o[1] = __float2bfloat16(a1 * inv); o[2] = __float2bfloat16(a2 * inv); o[3] = __float2bfloat16(a3 * inv);
      __builtin_amdgcn_fence(__ATOMIC_SEQ_CST, "wavefront");
    }
  }
}

extern "C" void kernel_launch(void* const* d_in, const int* in_sizes, int n_in, void* d_out, int out_size, void* d_ws, size_t ws_size,
                              hipStream_t stream) {
  mha_fwd_kernel<<<dim3(256), dim3(256), 0, stream>>>((const unsigned short*)d_in[0], (const unsigned short*)d_in[1],
                                                      (const unsigned short*)d_in[2], (unsigned short*)d_out);
}
```
